```python
import torch

def _softmax(x, axis=-1):
    return torch.softmax(x, dim=axis)
def _gelu(x):
    return torch.nn.functional.gelu(x, approximate="tanh")
def _silu(x):
    return torch.nn.functional.silu(x)
def _sigmoid(x):
    return torch.sigmoid(x)
def _layernorm(x, axis=-1, eps=1e-5):
    m = x.mean(dim=axis, keepdim=True)
    v = x.var(dim=axis, keepdim=True, unbiased=False)
    return (x - m) / torch.sqrt(v + eps)
def _scatter_add(tgt, idx, src, axis=0):
    return tgt.clone().index_add_(axis, idx, src)
def _segment_sum(src, seg, n):
    out = torch.zeros((n,) + src.shape[1:], dtype=src.dtype)
    return out.index_add_(0, seg, src)
def _pad(x, pw):
    flat = tuple(v for pair in reversed(pw) for v in pair)
    return torch.nn.functional.pad(x, flat)

kernel_name = 'gather_scatter__hybrid_graph_tf_graphgps__graph'

def setup_inputs():

    _w = (lambda a: (lambda t: t.to(torch.bfloat16) if t.is_floating_point() else t)(torch.as_tensor(a)))
    N, deg, D = 12500, 32, 128
    E = N * deg
    src = (lambda lo, hi, *s: torch.randint(lo, hi, s))(0, N, E).to(torch.int64)
    dst = (lambda lo, hi, *s: torch.randint(lo, hi, s))(0, N, E).to(torch.int64)
    deg_cnt = torch.bincount(dst, minlength=N).to(float) + 1.0
    sc = 1.0 / float(D) ** 0.5
    return {
        "x": _w((lambda *s: torch.randn(*s, dtype=torch.bfloat16) * 0.05)(N, D)),
        "src": _w(src),
        "dst": _w(dst),
        "deg_inv": _w((1.0 / deg_cnt).to(float)),
        "w_msg": _w((lambda *s: torch.randn(*s, dtype=torch.bfloat16) * 0.4)(D, D) * sc),
        "wq": _w((lambda *s: torch.randn(*s, dtype=torch.bfloat16) * 1.6)(D, D) * sc),
        "wk": _w((lambda *s: torch.randn(*s, dtype=torch.bfloat16) * 1.6)(D, D) * sc),
        "wv": _w((lambda *s: torch.randn(*s, dtype=torch.bfloat16) * 0.2)(D, D) * sc),
        "w_out": _w((lambda *s: torch.randn(*s, dtype=torch.bfloat16) * 0.2)(D, D) * sc),
    }

def reference(x, src, dst, deg_inv, w_msg, wq, wk, wv, w_out):
    N, D = x.shape[0], x.shape[1]
    h = _layernorm(x, axis=-1)
    m_e = torch.einsum('nd,de->ne', h, w_msg)
    h_local = _segment_sum(m_e[src], dst, N) * deg_inv[:, None]
    q = torch.einsum('nd,de->ne', h, wq)
    k = torch.einsum('nd,de->ne', h, wk)
    v = torch.einsum('nd,de->ne', h, wv)
    logits = torch.einsum('id,jd->ij', q, k) / (float(D) ** 0.5)
    attn = _softmax(logits, axis=-1)
    h_global = torch.einsum('ij,jd->id', attn, v)
    y = torch.einsum('nd,de->ne', h_local + h_global, w_out)
    return x + y
```

```cpp
#include <hip/hip_runtime.h>
#include <stdint.h>

typedef __attribute__((ext_vector_type(8))) __bf16 bf16x8;
typedef __attribute__((ext_vector_type(2))) __bf16 bf16x2;
typedef __attribute__((ext_vector_type(4))) short s16x4;
typedef __attribute__((ext_vector_type(2))) float f32x2;
typedef __attribute__((ext_vector_type(4))) float f32x4;
typedef __attribute__((ext_vector_type(2))) unsigned u32x2;
typedef __attribute__((ext_vector_type(4))) unsigned u32x4;
typedef __amdgpu_buffer_rsrc_t rsrc_t;
#define LDS_AS __attribute__((address_space(3)))
#define DEV __device__ __forceinline__

constexpr int NN = 12500;
constexpr int NQT = 784;
constexpr int NCH = 56, CH = 7144;
constexpr unsigned OOB = 0x7fff0000u, INVE = 0xffffu;
constexpr size_t OFF_H = 4096, OFF_Q = 4u << 20, OFF_E = 26u << 20, OFF_T = 28u << 20;

DEV rsrc_t mkrs(const void* p, unsigned bytes) { return __builtin_amdgcn_make_buffer_rsrc((void*)p, (short)0, (int)bytes, 0x00020000); }
DEV u32x4 ld16(rsrc_t r, unsigned off) { return __builtin_amdgcn_raw_buffer_load_b128(r, (int)off, 0, 0); }
DEV float bflo(unsigned v) { return __uint_as_float(v << 16); }
DEV unsigned pk2(float a, float b) { f32x2 f = {a, b}; return __builtin_bit_cast(unsigned, __builtin_convertvector(f, bf16x2)); }
DEV u32x4 pk8(f32x4 a, f32x4 b) { return (u32x4){pk2(a[0], a[1]), pk2(a[2], a[3]), pk2(b[0], b[1]), pk2(b[2], b[3])}; }
DEV f32x4 mfma(u32x4 a, u32x4 b, f32x4 c) { return __builtin_amdgcn_mfma_f32_16x16x32_bf16(__builtin_bit_cast(bf16x8, a), __builtin_bit_cast(bf16x8, b), c, 0, 0, 0); }
DEV float sum_g(float x) {
  auto s = __builtin_amdgcn_permlane16_swap(__float_as_uint(x), __float_as_uint(x), false, false);
  x = __uint_as_float(s[0]) + __uint_as_float(s[1]);
  auto t = __builtin_amdgcn_permlane32_swap(__float_as_uint(x), __float_as_uint(x), false, false);
  return __uint_as_float(t[0]) + __uint_as_float(t[1]);
}
DEV float max_g(float x) {
  auto s = __builtin_amdgcn_permlane16_swap(__float_as_uint(x), __float_as_uint(x), false, false);
  x = fmaxf(__uint_as_float(s[0]), __uint_as_float(s[1]));
  auto t = __builtin_amdgcn_permlane32_swap(__float_as_uint(x), __float_as_uint(x), false, false);
  return fmaxf(__uint_as_float(t[0]), __uint_as_float(t[1]));
}
DEV unsigned scan64(unsigned v) {
  v += __builtin_amdgcn_update_dpp(0u, v, 0x111, 0xf, 0xf, false);
  v += __builtin_amdgcn_update_dpp(0u, v, 0x112, 0xf, 0xf, false);
  v += __builtin_amdgcn_update_dpp(0u, v, 0x114, 0xf, 0xf, false);
  v += __builtin_amdgcn_update_dpp(0u, v, 0x118, 0xf, 0xf, false);
  v += __builtin_amdgcn_update_dpp(0u, v, 0x142, 0xa, 0xf, false);
  v += __builtin_amdgcn_update_dpp(0u, v, 0x143, 0xc, 0xf, false);
  return v;
}
DEV u32x2 trd(const char* sm, unsigned off) {
  return __builtin_bit_cast(u32x2, __builtin_amdgcn_ds_read_tr16_b64_v4i16((LDS_AS s16x4*)((LDS_AS char*)sm + off)));
}
DEV void dma16(rsrc_t r, unsigned voff, unsigned lds) {
  asm volatile("s_mov_b32 m0, %0\n\ts_nop 0\n\tbuffer_load_dwordx4 %1, %2, 0 offen lds" ::"s"(lds), "v"(voff), "s"(r) : "memory");
}
DEV unsigned ldsaddr(const void* p) { return (unsigned)(size_t)(LDS_AS const char*)p; }

__global__ __launch_bounds__(256, 1) void k1_prep(const unsigned short* __restrict__ x, const int* __restrict__ src, const int* __restrict__ dst,
                                                  const unsigned short* __restrict__ wm, const unsigned short* __restrict__ wq,
                                                  const unsigned short* __restrict__ wk, const unsigned short* __restrict__ wv,
                                                  const unsigned short* __restrict__ wo, char* __restrict__ ws) {
  __shared__ __attribute__((aligned(16))) char sm[90112];
  unsigned t = threadIdx.x;
  asm volatile(
      "s_cmp_lt_u32 %[b], 56\n"
      "s_cselect_b32 s64, 16, 0\n"
      "s_cselect_b32 s65, 8, 32\n"
      "s_load_dwordx2 s[40:41], %[ka], s64\n"
      "s_load_dwordx2 s[44:45], %[ka], s65\n"
      "s_load_dwordx2 s[48:49], %[ka], 40\n"
      "s_load_dwordx2 s[52:53], %[ka], 64\n"
      "s_load_dwordx2 s[56:57], %[ka], 64\n"
      "s_getpc_b64 s[60:61]\n"
      ".Lk1_pc:\n"
      "s_mov_b32 s62, .Lk2_asm_end-.Lk1_pc\n"
      "s_mov_b32 s63, 0x20000\n"
      "v_lshlrev_b32 v183, 7, %[t]\n"
      "v_lshlrev_b32 v184, 4, %[t]\n"
      "s_bfe_u32 s66, %[b], 0x40003\n"
      "v_and_b32 v183, 0x380, v183\n"
      "v_and_b32 v184, 0xc00, v184\n"
      "s_lshl_b32 s66, s66, 12\n"
      "v_or_b32 v183, v183, v184\n"
      "buffer_load_dword v184, v183, s[60:63], s66 offen\n"
      "s_cmp_lt_u32 %[b], 56\n"
      "s_cbranch_scc0 1f\n"
      "s_mov_b32 s42, 1600000\n"
      "s_mov_b32 s43, 0x20000\n"
      "s_mov_b32 s46, 1600000\n"
      "s_mov_b32 s47, 0x20000\n"
      "s_mov_b32 s58, 33554432\n"
      "s_mov_b32 s59, 0x20000\n"
      "s_mul_i32 s70, %[b], 28576\n"
      "s_add_u32 s71, s70, 4096\n"
      "s_add_u32 s72, s70, 8192\n"
      "s_add_u32 s73, s70, 12288\n"
      "s_add_u32 s74, s70, 16384\n"
      "s_add_u32 s75, s70, 20480\n"
      "s_add_u32 s76, s70, 24576\n"
      "v_lshlrev_b32 v140, 4, %[t]\n"
      "v_mov_b32 v144, 0\n"
      "v_mov_b32 v145, 0\n"
      "v_mov_b32 v146, 0\n"
      "v_mov_b32 v147, 0\n"
      "ds_write_b128 v140, v[144:147]\n"
      "s_waitcnt lgkmcnt(0)\n"
      "buffer_load_dwordx4 v[0:3], v140, s[40:43], s70 offen\n"
      "buffer_load_dwordx4 v[4:7], v140, s[40:43], s71 offen\n"
      "buffer_load_dwordx4 v[8:11], v140, s[40:43], s72 offen\n"
      "buffer_load_dwordx4 v[12:15], v140, s[40:43], s73 offen\n"
      "buffer_load_dwordx4 v[16:19], v140, s[40:43], s74 offen\n"
      "buffer_load_dwordx4 v[20:23], v140, s[40:43], s75 offen\n"
      "buffer_load_dwordx4 v[24:27], v140, s[40:43], s76 offen\n"
      "buffer_load_dwordx4 v[28:31], v140, s[44:47], s70 offen\n"
      "buffer_load_dwordx4 v[32:35], v140, s[44:47], s71 offen\n"
      "buffer_load_dwordx4 v[36:39], v140, s[44:47], s72 offen\n"
      "buffer_load_dwordx4 v[40:43], v140, s[44:47], s73 offen\n"
      "buffer_load_dwordx4 v[44:47], v140, s[44:47], s74 offen\n"
      "buffer_load_dwordx4 v[48:51], v140, s[44:47], s75 offen\n"
      "buffer_load_dwordx4 v[52:55], v140, s[44:47], s76 offen\n"
      "s_add_u32 s70, s70, 27262976\n"
      "s_add_u32 s71, s71, 27262976\n"
      "s_add_u32 s72, s72, 27262976\n"
      "s_add_u32 s73, s73, 27262976\n"
      "s_add_u32 s74, s74, 27262976\n"
      "s_add_u32 s75, s75, 27262976\n"
      "s_add_u32 s76, s76, 27262976\n"
      "s_and_b32 s68, %[b], 7\n"
      "s_lshl_b32 s68, s68, 7\n"
      "s_lshr_b32 s69, %[b], 3\n"
      "s_lshl_b32 s69, s69, 4\n"
      "s_add_u32 s68, s68, s69\n"
      "s_add_u32 s68, s68, 29360128\n"
      "v_mul_u32_u24 v153, 1024, %[t]\n"
      "s_barrier\n"
      "v_readfirstlane_b32 s64, %[t]\n"
      "s_sub_u32 s65, 28862976, s76\n"
      "s_min_u32 s65, s65, 4000\n"
      "v_cmp_gt_u32 s[66:67], s65, v140\n"
      "v_mov_b32 v141, 4\n"
      "v_mov_b32 v142, 4092\n"
      "s_movk_i32 s69, 20165\n"
      "s_mov_b32 s77, 0x10000\n"
      "s_lshr_b32 s64, s64, 6\n"
      "v_mov_b32 v143, 0x7fff0000\n"
      "v_cndmask_b32 v143, v143, v140, s[66:67]\n"
      "s_waitcnt vmcnt(13)\n"
      "v_lshrrev_b32 v56, 8, v0\n"
      "v_lshrrev_b32 v57, 8, v1\n"
      "v_lshrrev_b32 v58, 8, v2\n"
      "v_lshrrev_b32 v59, 8, v3\n"
      "v_lshl_add_u32 v56, v56, 2, v0\n"
      "v_lshl_add_u32 v57, v57, 2, v1\n"
      "v_lshl_add_u32 v58, v58, 2, v2\n"
      "v_lshl_add_u32 v59, v59, 2, v3\n"
      "v_mul_u32_u24 v0, s69, v56\n"
      "v_mul_u32_u24 v1, s69, v57\n"
      "v_mul_u32_u24 v2, s69, v58\n"
      "v_mul_u32_u24 v3, s69, v59\n"
      "v_lshrrev_b32 v0, 18, v0\n"
      "v_lshrrev_b32 v1, 18, v1\n"
      "v_lshrrev_b32 v2, 18, v2\n"
      "v_lshrrev_b32 v3, 18, v3\n"
      "v_lshlrev_b32 v84, 2, v0\n"
      "ds_add_rtn_u32 v112, v84, v141\n"
      "v_lshlrev_b32 v85, 2, v1\n"
      "ds_add_rtn_u32 v113, v85, v141\n"
      "v_lshlrev_b32 v86, 2, v2\n"
      "ds_add_rtn_u32 v114, v86, v141\n"
      "v_lshlrev_b32 v87, 2, v3\n"
      "ds_add_rtn_u32 v115, v87, v141\n"
      "s_waitcnt vmcnt(12)\n"
      "v_lshrrev_b32 v60, 8, v4\n"
      "v_lshrrev_b32 v61, 8, v5\n"
      "v_lshrrev_b32 v62, 8, v6\n"
      "v_lshrrev_b32 v63, 8, v7\n"
      "v_lshl_add_u32 v60, v60, 2, v4\n"
      "v_lshl_add_u32 v61, v61, 2, v5\n"
      "v_lshl_add_u32 v62, v62, 2, v6\n"
      "v_lshl_add_u32 v63, v63, 2, v7\n"
      "v_mul_u32_u24 v4, s69, v60\n"
      "v_mul_u32_u24 v5, s69, v61\n"
      "v_mul_u32_u24 v6, s69, v62\n"
      "v_mul_u32_u24 v7, s69, v63\n"
      "v_lshrrev_b32 v4, 18, v4\n"
      "v_lshrrev_b32 v5, 18, v5\n"
      "v_lshrrev_b32 v6, 18, v6\n"
      "v_lshrrev_b32 v7, 18, v7\n"
      "v_lshlrev_b32 v88, 2, v4\n"
      "ds_add_rtn_u32 v116, v88, v141\n"
      "v_lshlrev_b32 v89, 2, v5\n"
      "ds_add_rtn_u32 v117, v89, v141\n"
      "v_lshlrev_b32 v90, 2, v6\n"
      "ds_add_rtn_u32 v118, v90, v141\n"
      "v_lshlrev_b32 v91, 2, v7\n"
      "ds_add_rtn_u32 v119, v91, v141\n"
      "s_waitcnt vmcnt(11)\n"
      "v_lshrrev_b32 v64, 8, v8\n"
      "v_lshrrev_b32 v65, 8, v9\n"
      "v_lshrrev_b32 v66, 8, v10\n"
      "v_lshrrev_b32 v67, 8, v11\n"
      "v_lshl_add_u32 v64, v64, 2, v8\n"
      "v_lshl_add_u32 v65, v65, 2, v9\n"
      "v_lshl_add_u32 v66, v66, 2, v10\n"
      "v_lshl_add_u32 v67, v67, 2, v11\n"
      "v_mul_u32_u24 v8, s69, v64\n"
      "v_mul_u32_u24 v9, s69, v65\n"
      "v_mul_u32_u24 v10, s69, v66\n"
      "v_mul_u32_u24 v11, s69, v67\n"
      "v_lshrrev_b32 v8, 18, v8\n"
      "v_lshrrev_b32 v9, 18, v9\n"
      "v_lshrrev_b32 v10, 18, v10\n"
      "v_lshrrev_b32 v11, 18, v11\n"
      "v_lshlrev_b32 v92, 2, v8\n"
      "ds_add_rtn_u32 v120, v92, v141\n"
      "v_lshlrev_b32 v93, 2, v9\n"
      "ds_add_rtn_u32 v121, v93, v141\n"
      "v_lshlrev_b32 v94, 2, v10\n"
      "ds_add_rtn_u32 v122, v94, v141\n"
      "v_lshlrev_b32 v95, 2, v11\n"
      "ds_add_rtn_u32 v123, v95, v141\n"
      "s_waitcnt vmcnt(10)\n"
      "v_lshrrev_b32 v68, 8, v12\n"
      "v_lshrrev_b32 v69, 8, v13\n"
      "v_lshrrev_b32 v70, 8, v14\n"
      "v_lshrrev_b32 v71, 8, v15\n"
      "v_lshl_add_u32 v68, v68, 2, v12\n"
      "v_lshl_add_u32 v69, v69, 2, v13\n"
      "v_lshl_add_u32 v70, v70, 2, v14\n"
      "v_lshl_add_u32 v71, v71, 2, v15\n"
      "v_mul_u32_u24 v12, s69, v68\n"
      "v_mul_u32_u24 v13, s69, v69\n"
      "v_mul_u32_u24 v14, s69, v70\n"
      "v_mul_u32_u24 v15, s69, v71\n"
      "v_lshrrev_b32 v12, 18, v12\n"
      "v_lshrrev_b32 v13, 18, v13\n"
      "v_lshrrev_b32 v14, 18, v14\n"
      "v_lshrrev_b32 v15, 18, v15\n"
      "v_lshlrev_b32 v96, 2, v12\n"
      "ds_add_rtn_u32 v124, v96, v141\n"
      "v_lshlrev_b32 v97, 2, v13\n"
      "ds_add_rtn_u32 v125, v97, v141\n"
      "v_lshlrev_b32 v98, 2, v14\n"
      "ds_add_rtn_u32 v126, v98, v141\n"
      "v_lshlrev_b32 v99, 2, v15\n"
      "ds_add_rtn_u32 v127, v99, v141\n"
      "s_waitcnt vmcnt(9)\n"
      "v_lshrrev_b32 v72, 8, v16\n"
      "v_lshrrev_b32 v73, 8, v17\n"
      "v_lshrrev_b32 v74, 8, v18\n"
      "v_lshrrev_b32 v75, 8, v19\n"
      "v_lshl_add_u32 v72, v72, 2, v16\n"
      "v_lshl_add_u32 v73, v73, 2, v17\n"
      "v_lshl_add_u32 v74, v74, 2, v18\n"
      "v_lshl_add_u32 v75, v75, 2, v19\n"
      "v_mul_u32_u24 v16, s69, v72\n"
      "v_mul_u32_u24 v17, s69, v73\n"
      "v_mul_u32_u24 v18, s69, v74\n"
      "v_mul_u32_u24 v19, s69, v75\n"
      "v_lshrrev_b32 v16, 18, v16\n"
      "v_lshrrev_b32 v17, 18, v17\n"
      "v_lshrrev_b32 v18, 18, v18\n"
      "v_lshrrev_b32 v19, 18, v19\n"
      "v_lshlrev_b32 v100, 2, v16\n"
      "ds_add_rtn_u32 v128, v100, v141\n"
      "v_lshlrev_b32 v101, 2, v17\n"
      "ds_add_rtn_u32 v129, v101, v141\n"
      "v_lshlrev_b32 v102, 2, v18\n"
      "ds_add_rtn_u32 v130, v102, v141\n"
      "v_lshlrev_b32 v103, 2, v19\n"
      "ds_add_rtn_u32 v131, v103, v141\n"
      "s_waitcnt vmcnt(8)\n"
      "v_lshrrev_b32 v76, 8, v20\n"
      "v_lshrrev_b32 v77, 8, v21\n"
      "v_lshrrev_b32 v78, 8, v22\n"
      "v_lshrrev_b32 v79, 8, v23\n"
      "v_lshl_add_u32 v76, v76, 2, v20\n"
      "v_lshl_add_u32 v77, v77, 2, v21\n"
      "v_lshl_add_u32 v78, v78, 2, v22\n"
      "v_lshl_add_u32 v79, v79, 2, v23\n"
      "v_mul_u32_u24 v20, s69, v76\n"
      "v_mul_u32_u24 v21, s69, v77\n"
      "v_mul_u32_u24 v22, s69, v78\n"
      "v_mul_u32_u24 v23, s69, v79\n"
      "v_lshrrev_b32 v20, 18, v20\n"
      "v_lshrrev_b32 v21, 18, v21\n"
      "v_lshrrev_b32 v22, 18, v22\n"
      "v_lshrrev_b32 v23, 18, v23\n"
      "v_lshlrev_b32 v104, 2, v20\n"
      "ds_add_rtn_u32 v132, v104, v141\n"
      "v_lshlrev_b32 v105, 2, v21\n"
      "ds_add_rtn_u32 v133, v105, v141\n"
      "v_lshlrev_b32 v106, 2, v22\n"
      "ds_add_rtn_u32 v134, v106, v141\n"
      "v_lshlrev_b32 v107, 2, v23\n"
      "ds_add_rtn_u32 v135, v107, v141\n"
      "s_waitcnt vmcnt(7)\n"
      "v_lshrrev_b32 v80, 8, v24\n"
      "v_lshrrev_b32 v81, 8, v25\n"
      "v_lshrrev_b32 v82, 8, v26\n"
      "v_lshrrev_b32 v83, 8, v27\n"
      "v_lshl_add_u32 v80, v80, 2, v24\n"
      "v_lshl_add_u32 v81, v81, 2, v25\n"
      "v_lshl_add_u32 v82, v82, 2, v26\n"
      "v_lshl_add_u32 v83, v83, 2, v27\n"
      "v_mul_u32_u24 v24, s69, v80\n"
      "v_mul_u32_u24 v25, s69, v81\n"
      "v_mul_u32_u24 v26, s69, v82\n"
      "v_mul_u32_u24 v27, s69, v83\n"
      "v_lshrrev_b32 v24, 18, v24\n"
      "v_lshrrev_b32 v25, 18, v25\n"
      "v_lshrrev_b32 v26, 18, v26\n"
      "v_lshrrev_b32 v27, 18, v27\n"
      "v_lshlrev_b32 v108, 2, v24\n"
      "v_cndmask_b32 v108, v142, v108, s[66:67]\n"
      "ds_add_rtn_u32 v136, v108, v141\n"
      "v_lshlrev_b32 v109, 2, v25\n"
      "v_cndmask_b32 v109, v142, v109, s[66:67]\n"
      "ds_add_rtn_u32 v137, v109, v141\n"
      "v_lshlrev_b32 v110, 2, v26\n"
      "v_cndmask_b32 v110, v142, v110, s[66:67]\n"
      "ds_add_rtn_u32 v138, v110, v141\n"
      "v_lshlrev_b32 v111, 2, v27\n"
      "v_cndmask_b32 v111, v142, v111, s[66:67]\n"
      "ds_add_rtn_u32 v139, v111, v141\n"
      "v_mad_i32_i24 v56, v0, -13, v56\n"
      "v_mad_i32_i24 v57, v1, -13, v57\n"
      "v_mad_i32_i24 v58, v2, -13, v58\n"
      "v_mad_i32_i24 v59, v3, -13, v59\n"
      "v_mad_i32_i24 v60, v4, -13, v60\n"
      "v_mad_i32_i24 v61, v5, -13, v61\n"
      "v_mad_i32_i24 v62, v6, -13, v62\n"
      "v_mad_i32_i24 v63, v7, -13, v63\n"
      "s_waitcnt lgkmcnt(0)\n"
      "s_barrier\n"
      "ds_read_b128 v[144:147], v140\n"
      "v_mad_i32_i24 v64, v8, -13, v64\n"
      "v_mad_i32_i24 v65, v9, -13, v65\n"
      "v_mad_i32_i24 v66, v10, -13, v66\n"
      "v_mad_i32_i24 v67, v11, -13, v67\n"
      "v_mad_i32_i24 v68, v12, -13, v68\n"
      "v_mad_i32_i24 v69, v13, -13, v69\n"
      "v_mad_i32_i24 v70, v14, -13, v70\n"
      "v_mad_i32_i24 v71, v15, -13, v71\n"
      "v_mad_i32_i24 v72, v16, -13, v72\n"
      "v_mad_i32_i24 v73, v17, -13, v73\n"
      "v_mad_i32_i24 v74, v18, -13, v74\n"
      "v_mad_i32_i24 v75, v19, -13, v75\n"
      "s_waitcnt lgkmcnt(0)\n"
      "v_add_u32 v148, v144, v145\n"
      "v_add3_u32 v148, v148, v146, v147\n"
      "v_mov_b32 v149, v148\n"
      "v_mad_i32_i24 v76, v20, -13, v76\n"
      "v_mad_i32_i24 v77, v21, -13, v77\n"
      "v_add_u32_dpp v149, v149, v149 row_shr:1 row_mask:0xf bank_mask:0xf bound_ctrl:0\n"
      "v_mad_i32_i24 v78, v22, -13, v78\n"
      "v_mad_i32_i24 v79, v23, -13, v79\n"
      "v_add_u32_dpp v149, v149, v149 row_shr:2 row_mask:0xf bank_mask:0xf bound_ctrl:0\n"
      "v_mad_i32_i24 v80, v24, -13, v80\n"
      "v_mad_i32_i24 v81, v25, -13, v81\n"
      "v_add_u32_dpp v149, v149, v149 row_shr:4 row_mask:0xf bank_mask:0xf bound_ctrl:0\n"
      "v_mad_i32_i24 v82, v26, -13, v82\n"
      "v_mad_i32_i24 v83, v27, -13, v83\n"
      "v_add_u32_dpp v149, v149, v149 row_shr:8 row_mask:0xf bank_mask:0xf bound_ctrl:0\n"
      "s_waitcnt vmcnt(6)\n"
      "v_lshl_or_b32 v28, s77, v56, v28\n"
      "v_lshl_or_b32 v29, s77, v57, v29\n"
      "v_add_u32_dpp v149, v149, v149 row_bcast:15 row_mask:0xa bank_mask:0xf\n"
      "v_lshl_or_b32 v30, s77, v58, v30\n"
      "v_lshl_or_b32 v31, s77, v59, v31\n"
      "v_add_u32_dpp v149, v149, v149 row_bcast:31 row_mask:0xc bank_mask:0xf\n"
      "v_lshrrev_b32 v150, 2, v140\n"
      "ds_write_b32 v150, v149 offset:4096\n"
      "v_mov_b32 v151, 4096\n"
      "s_waitcnt vmcnt(5)\n"
      "v_lshl_or_b32 v32, s77, v60, v32\n"
      "v_lshl_or_b32 v33, s77, v61, v33\n"
      "v_lshl_or_b32 v34, s77, v62, v34\n"
      "v_lshl_or_b32 v35, s77, v63, v35\n"
      "s_waitcnt lgkmcnt(0)\n"
      "s_barrier\n"
      "ds_read2_b32 v[154:155], v151 offset0:63 offset1:127\n"
      "ds_read_b32 v156, v151 offset:764\n"
      "s_waitcnt vmcnt(4)\n"
      "v_lshl_or_b32 v36, s77, v64, v36\n"
      "v_lshl_or_b32 v37, s77, v65, v37\n"
      "v_lshl_or_b32 v38, s77, v66, v38\n"
      "v_lshl_or_b32 v39, s77, v67, v39\n"
      "s_waitcnt vmcnt(3)\n"
      "v_lshl_or_b32 v40, s77, v68, v40\n"
      "v_lshl_or_b32 v41, s77, v69, v41\n"
      "v_lshl_or_b32 v42, s77, v70, v42\n"
      "v_lshl_or_b32 v43, s77, v71, v43\n"
      "s_waitcnt vmcnt(2)\n"
      "v_lshl_or_b32 v44, s77, v72, v44\n"
      "v_lshl_or_b32 v45, s77, v73, v45\n"
      "s_cmp_gt_u32 s64, 0\n"
      "s_cselect_b32 s78, -1, 0\n"
      "s_cmp_gt_u32 s64, 1\n"
      "s_cselect_b32 s79, -1, 0\n"
      "s_cmp_gt_u32 s64, 2\n"
      "s_cselect_b32 s80, -1, 0\n"
      "v_sub_u32 v149, v149, v148\n"
      "s_waitcnt lgkmcnt(1)\n"
      "v_and_b32 v154, s78, v154\n"
      "v_and_b32 v155, s79, v155\n"
      "s_waitcnt lgkmcnt(0)\n"
      "v_and_b32 v156, s80, v156\n"
      "v_add3_u32 v149, v149, v154, v155\n"
      "v_add_u32 v156, v149, v156\n"
      "v_add_u32 v157, v156, v144\n"
      "v_add_u32 v158, v157, v145\n"
      "v_add_u32 v159, v158, v146\n"
      "ds_write_b128 v140, v[156:159]\n"
      "v_lshl_or_b32 v144, v144, 16, v156\n"
      "v_lshl_or_b32 v145, v145, 16, v157\n"
      "v_lshl_or_b32 v146, v146, 16, v158\n"
      "v_lshl_or_b32 v147, v147, 16, v159\n"
      "buffer_store_dwordx4 v[144:147], v153, s[56:59], s68 offen\n"
      "v_lshl_or_b32 v46, s77, v74, v46\n"
      "v_lshl_or_b32 v47, s77, v75, v47\n"
      "s_waitcnt vmcnt(2)\n"
      "v_lshl_or_b32 v48, s77, v76, v48\n"
      "v_lshl_or_b32 v49, s77, v77, v49\n"
      "v_lshl_or_b32 v50, s77, v78, v50\n"
      "v_lshl_or_b32 v51, s77, v79, v51\n"
      "s_waitcnt vmcnt(1)\n"
      "v_lshl_or_b32 v52, s77, v80, v52\n"
      "v_lshl_or_b32 v53, s77, v81, v53\n"
      "v_lshl_or_b32 v54, s77, v82, v54\n"
      "v_lshl_or_b32 v55, s77, v83, v55\n"
      "s_waitcnt lgkmcnt(0)\n"
      "s_barrier\n"
      "ds_read_b32 v84, v84\n"
      "ds_read_b32 v85, v85\n"
      "ds_read_b32 v86, v86\n"
      "ds_read_b32 v87, v87\n"
      "ds_read_b32 v88, v88\n"
      "ds_read_b32 v89, v89\n"
      "ds_read_b32 v90, v90\n"
      "ds_read_b32 v91, v91\n"
      "ds_read_b32 v92, v92\n"
      "ds_read_b32 v93, v93\n"
      "ds_read_b32 v94, v94\n"
      "ds_read_b32 v95, v95\n"
      "ds_read_b32 v96, v96\n"
      "ds_read_b32 v97, v97\n"
      "ds_read_b32 v98, v98\n"
      "ds_read_b32 v99, v99\n"
      "ds_read_b32 v100, v100\n"
      "ds_read_b32 v101, v101\n"
      "ds_read_b32 v102, v102\n"
      "ds_read_b32 v103, v103\n"
      "ds_read_b32 v104, v104\n"
      "ds_read_b32 v105, v105\n"
      "ds_read_b32 v106, v106\n"
      "ds_read_b32 v107, v107\n"
      "ds_read_b32 v108, v108\n"
      "ds_read_b32 v109, v109\n"
      "ds_read_b32 v110, v110\n"
      "ds_read_b32 v111, v111\n"
      "s_waitcnt lgkmcnt(14)\n"
      "v_add_u32 v112, v112, v84\n"
      "ds_write_b32 v112, v28 offset:8192\n"
      "v_add_u32 v113, v113, v85\n"
      "ds_write_b32 v113, v29 offset:8192\n"
      "v_add_u32 v114, v114, v86\n"
      "ds_write_b32 v114, v30 offset:8192\n"
      "v_add_u32 v115, v115, v87\n"
      "ds_write_b32 v115, v31 offset:8192\n"
      "v_add_u32 v116, v116, v88\n"
      "ds_write_b32 v116, v32 offset:8192\n"
      "v_add_u32 v117, v117, v89\n"
      "ds_write_b32 v117, v33 offset:8192\n"
      "v_add_u32 v118, v118, v90\n"
      "ds_write_b32 v118, v34 offset:8192\n"
      "v_add_u32 v119, v119, v91\n"
      "ds_write_b32 v119, v35 offset:8192\n"
      "v_add_u32 v120, v120, v92\n"
      "ds_write_b32 v120, v36 offset:8192\n"
      "v_add_u32 v121, v121, v93\n"
      "ds_write_b32 v121, v37 offset:8192\n"
      "v_add_u32 v122, v122, v94\n"
      "ds_write_b32 v122, v38 offset:8192\n"
      "v_add_u32 v123, v123, v95\n"
      "ds_write_b32 v123, v39 offset:8192\n"
      "v_add_u32 v124, v124, v96\n"
      "ds_write_b32 v124, v40 offset:8192\n"
      "v_add_u32 v125, v125, v97\n"
      "ds_write_b32 v125, v41 offset:8192\n"
      "s_waitcnt lgkmcnt(14)\n"
      "v_add_u32 v126, v126, v98\n"
      "ds_write_b32 v126, v42 offset:8192\n"
      "v_add_u32 v127, v127, v99\n"
      "ds_write_b32 v127, v43 offset:8192\n"
      "v_add_u32 v128, v128, v100\n"
      "ds_write_b32 v128, v44 offset:8192\n"
      "v_add_u32 v129, v129, v101\n"
      "ds_write_b32 v129, v45 offset:8192\n"
      "v_add_u32 v130, v130, v102\n"
      "ds_write_b32 v130, v46 offset:8192\n"
      "v_add_u32 v131, v131, v103\n"
      "ds_write_b32 v131, v47 offset:8192\n"
      "v_add_u32 v132, v132, v104\n"
      "ds_write_b32 v132, v48 offset:8192\n"
      "v_add_u32 v133, v133, v105\n"
      "ds_write_b32 v133, v49 offset:8192\n"
      "v_add_u32 v134, v134, v106\n"
      "ds_write_b32 v134, v50 offset:8192\n"
      "v_add_u32 v135, v135, v107\n"
      "ds_write_b32 v135, v51 offset:8192\n"
      "v_add_u32 v136, v136, v108\n"
      "ds_write_b32 v136, v52 offset:8192\n"
      "v_add_u32 v137, v137, v109\n"
      "ds_write_b32 v137, v53 offset:8192\n"
      "v_add_u32 v138, v138, v110\n"
      "ds_write_b32 v138, v54 offset:8192\n"
      "v_add_u32 v139, v139, v111\n"
      "ds_write_b32 v139, v55 offset:8192\n"
      "s_waitcnt lgkmcnt(0)\n"
      "s_barrier\n"
      "ds_read_b128 v[0:3], v140 offset:8192\n"
      "ds_read_b128 v[4:7], v140 offset:12288\n"
      "ds_read_b128 v[8:11], v140 offset:16384\n"
      "ds_read_b128 v[12:15], v140 offset:20480\n"
      "ds_read_b128 v[16:19], v140 offset:24576\n"
      "ds_read_b128 v[20:23], v140 offset:28672\n"
      "ds_read_b128 v[24:27], v140 offset:32768\n"
      "s_waitcnt lgkmcnt(6)\n"
      "buffer_store_dwordx4 v[0:3], v140, s[56:59], s70 offen sc1\n"
      "s_waitcnt lgkmcnt(5)\n"
      "buffer_store_dwordx4 v[4:7], v140, s[56:59], s71 offen sc1\n"
      "s_waitcnt lgkmcnt(4)\n"
      "buffer_store_dwordx4 v[8:11], v140, s[56:59], s72 offen sc1\n"
      "s_waitcnt lgkmcnt(3)\n"
      "buffer_store_dwordx4 v[12:15], v140, s[56:59], s73 offen sc1\n"
      "s_waitcnt lgkmcnt(2)\n"
      "buffer_store_dwordx4 v[16:19], v140, s[56:59], s74 offen sc1\n"
      "s_waitcnt lgkmcnt(1)\n"
      "buffer_store_dwordx4 v[20:23], v140, s[56:59], s75 offen sc1\n"
      "s_waitcnt lgkmcnt(0)\n"
      "buffer_store_dwordx4 v[24:27], v143, s[56:59], s76 offen sc1\n"
      "s_endpgm\n"
      "1:\n"
      "s_cmp_lt_u32 %[b], 60\n"
      "s_cbranch_scc1 2f\n"
      "v_readfirstlane_b32 s64, %[t]\n"
      "v_lshlrev_b32 v170, 6, %[t]\n"
      "v_and_b32 v171, 0x700, v170\n"
      "v_lshlrev_b32 v172, 1, %[t]\n"
      "v_and_or_b32 v171, v172, 64, v171\n"
      "v_lshlrev_b32 v175, 4, %[t]\n"
      "v_and_b32 v173, 16, %[t]\n"
      "v_and_b32 v174, 48, v175\n"
      "s_lshr_b32 s64, s64, 6\n"
      "s_lshl_b32 s65, s64, 13\n"
      "v_xor_b32 v173, v173, v174\n"
      "v_add3_u32 v176, v171, v173, s65\n"
      "v_xor_b32 v177, 32, v176\n"
      "s_movk_i32 s66, 0x1000\n"
      "v_and_b32 v175, 0x3f0, v175\n"
      "s_mov_b32 s42, 3200000\n"
      "s_mov_b32 s43, 0x20000\n"
      "s_mov_b32 s46, 32768\n"
      "s_mov_b32 s47, 0x20000\n"
      "s_mov_b32 s50, 32768\n"
      "s_mov_b32 s51, 0x20000\n"
      "s_lshl_b32 s67, %[b], 2\n"
      "s_add_u32 s67, s67, s64\n"
      "s_sub_u32 s67, s67, 240\n"
      "s_lshl_b32 s67, s67, 12\n"
      "v_lshlrev_b32 v182, 8, %[t]\n"
      "v_and_b32 v182, 0xf00, v182\n"
      "v_and_b32 v173, 48, %[t]\n"
      "v_add3_u32 v182, v182, v173, s67\n"
      "s_add_u32 m0, s65, 0\n"
      "s_waitcnt lgkmcnt(0)\n"
      "buffer_load_dwordx4 v176, s[44:47], 0 offen offset:0 lds\n"
      "buffer_load_dwordx4 v177, s[44:47], 0 offen offset:2048 lds\n"
      "s_add_u32 m0, s65, 896\n"
      "s_nop 0\n"
      "buffer_load_dwordx4 v176, s[44:47], 0 offen offset:128 lds\n"
      "buffer_load_dwordx4 v177, s[44:47], 0 offen offset:2176 lds\n"
      "s_add_u32 m0, s65, 4096\n"
      "s_nop 0\n"
      "buffer_load_dwordx4 v176, s[44:47], s66 offen offset:0 lds\n"
      "buffer_load_dwordx4 v177, s[44:47], s66 offen offset:2048 lds\n"
      "s_add_u32 m0, s65, 4992\n"
      "s_nop 0\n"
      "buffer_load_dwordx4 v176, s[44:47], s66 offen offset:128 lds\n"
      "buffer_load_dwordx4 v177, s[44:47], s66 offen offset:2176 lds\n"
      "buffer_load_dwordx4 v[0:3], v182, s[40:43], 0 offen offset:0\n"
      "buffer_load_dwordx4 v[4:7], v182, s[40:43], 0 offen offset:64\n"
      "buffer_load_dwordx4 v[8:11], v182, s[40:43], 0 offen offset:128\n"
      "buffer_load_dwordx4 v[12:15], v182, s[40:43], 0 offen offset:192\n"
      "s_add_u32 m0, s65, 32768\n"
      "s_nop 0\n"
      "buffer_load_dwordx4 v176, s[48:51], 0 offen offset:0 lds\n"
      "buffer_load_dwordx4 v177, s[48:51], 0 offen offset:2048 lds\n"
      "s_add_u32 m0, s65, 33664\n"
      "s_nop 0\n"
      "buffer_load_dwordx4 v176, s[48:51], 0 offen offset:128 lds\n"
      "buffer_load_dwordx4 v177, s[48:51], 0 offen offset:2176 lds\n"
      "s_add_u32 m0, s65, 36864\n"
      "s_nop 0\n"
      "buffer_load_dwordx4 v176, s[48:51], s66 offen offset:0 lds\n"
      "buffer_load_dwordx4 v177, s[48:51], s66 offen offset:2048 lds\n"
      "s_add_u32 m0, s65, 37760\n"
      "s_nop 0\n"
      "buffer_load_dwordx4 v176, s[48:51], s66 offen offset:128 lds\n"
      "buffer_load_dwordx4 v177, s[48:51], s66 offen offset:2176 lds\n"
      "s_mov_b32 s54, 3204096\n"
      "s_mov_b32 s55, 0x20000\n"
      "s_mov_b32 s58, 33554432\n"
      "s_mov_b32 s59, 0x20000\n"
      "s_add_u32 s67, s67, 4194304\n"
      "s_sub_u32 s68, %[b], 60\n"
      "s_lshr_b32 s69, s68, 2\n"
      "s_mul_i32 s69, s69, 332800\n"
      "s_mul_i32 s68, s68, 1280\n"
      "s_add_u32 s68, s68, s69\n"
      "s_add_u32 s68, s68, 8721408\n"
      "v_cmp_gt_u32 vcc, 80, %[t]\n"
      "v_lshlrev_b32 v185, 4, %[t]\n"
      "v_mov_b32 v186, 0x7fff0000\n"
      "v_cndmask_b32 v185, v186, v185, vcc\n"
      "v_mov_b32 v186, 0\n"
      "v_mov_b32 v187, 0\n"
      "v_mov_b32 v188, 0\n"
      "v_mov_b32 v189, 0\n"
      "buffer_store_dwordx4 v[186:189], v185, s[56:59], s68 offen sc1\n"
      "v_lshlrev_b32 v178, 7, %[t]\n"
      "v_and_b32 v178, 0x1800, v178\n"
      "v_and_b32 v179, 0xf0, v175\n"
      "v_and_b32 v172, 32, v172\n"
      "v_xor_b32 v179, v179, v172\n"
      "v_or_b32 v178, v178, v179\n"
      "v_xor_b32 v180, 16, v178\n"
      "v_lshlrev_b32 v179, 9, %[t]\n"
      "v_and_b32 v179, 0x1800, v179\n"
      "v_and_b32 v170, 0xc0, v170\n"
      "v_lshlrev_b32 v171, 3, %[t]\n"
      "v_and_b32 v171, 32, v171\n"
      "v_xor_b32 v171, v171, v173\n"
      "v_or3_b32 v179, v179, v170, v171\n"
      "v_xor_b32 v181, 16, v179\n"
      "s_mov_b32 vcc_lo, 0\n"
      "s_mov_b32 vcc_hi, 0xffff0000\n"
      "s_waitcnt vmcnt(12)\n"
      "v_lshlrev_b32 v16, 16, v0\n"
      "v_and_b32 v17, 0xffff0000, v0\n"
      "v_lshlrev_b32 v18, 16, v1\n"
      "v_and_b32 v19, 0xffff0000, v1\n"
      "v_lshlrev_b32 v20, 16, v2\n"
      "v_and_b32 v21, 0xffff0000, v2\n"
      "v_lshlrev_b32 v22, 16, v3\n"
      "v_and_b32 v23, 0xffff0000, v3\n"
      "s_waitcnt vmcnt(11)\n"
      "v_lshlrev_b32 v24, 16, v4\n"
      "v_and_b32 v25, 0xffff0000, v4\n"
      "v_lshlrev_b32 v26, 16, v5\n"
      "v_and_b32 v27, 0xffff0000, v5\n"
      "v_lshlrev_b32 v28, 16, v6\n"
      "v_and_b32 v29, 0xffff0000, v6\n"
      "v_lshlrev_b32 v30, 16, v7\n"
      "v_and_b32 v31, 0xffff0000, v7\n"
      "s_waitcnt vmcnt(10)\n"
      "v_lshlrev_b32 v32, 16, v8\n"
      "v_and_b32 v33, 0xffff0000, v8\n"
      "v_lshlrev_b32 v34, 16, v9\n"
      "v_and_b32 v35, 0xffff0000, v9\n"
      "v_lshlrev_b32 v36, 16, v10\n"
      "v_and_b32 v37, 0xffff0000, v10\n"
      "v_lshlrev_b32 v38, 16, v11\n"
      "v_and_b32 v39, 0xffff0000, v11\n"
      "s_waitcnt vmcnt(9)\n"
      "v_lshlrev_b32 v40, 16, v12\n"
      "v_and_b32 v41, 0xffff0000, v12\n"
      "v_lshlrev_b32 v42, 16, v13\n"
      "v_and_b32 v43, 0xffff0000, v13\n"
      "v_lshlrev_b32 v44, 16, v14\n"
      "v_and_b32 v45, 0xffff0000, v14\n"
      "v_lshlrev_b32 v46, 16, v15\n"
      "v_and_b32 v47, 0xffff0000, v15\n"
      "v_pk_add_f32 v[144:145], v[16:17], v[18:19]\n"
      "v_pk_add_f32 v[146:147], v[20:21], v[22:23]\n"
      "v_pk_add_f32 v[148:149], v[24:25], v[26:27]\n"
      "v_pk_add_f32 v[150:151], v[28:29], v[30:31]\n"
      "v_pk_add_f32 v[144:145], v[144:145], v[32:33]\n"
      "v_pk_add_f32 v[146:147], v[146:147], v[34:35]\n"
      "v_pk_add_f32 v[148:149], v[148:149], v[36:37]\n"
      "v_pk_add_f32 v[150:151], v[150:151], v[38:39]\n"
      "v_pk_add_f32 v[144:145], v[144:145], v[40:41]\n"
      "v_pk_add_f32 v[146:147], v[146:147], v[42:43]\n"
      "v_pk_add_f32 v[148:149], v[148:149], v[44:45]\n"
      "v_pk_add_f32 v[150:151], v[150:151], v[46:47]\n"
      "v_pk_add_f32 v[144:145], v[144:145], v[146:147]\n"
      "v_pk_add_f32 v[148:149], v[148:149], v[150:151]\n"
      "s_waitcnt vmcnt(9)\n"
      "s_barrier\n"
      "v_pk_add_f32 v[144:145], v[144:145], v[148:149]\n"
      "ds_read_b64_tr_b16 a[0:1], v178 offset:0\n"
      "v_add_f32 v144, v144, v145\n"
      "ds_read_b64_tr_b16 a[2:3], v180 offset:256\n"
      "v_mov_b32 v145, v144\n"
      "ds_read_b64_tr_b16 a[4:5], v178 offset:8192\n"
      "s_nop 0\n"
      "v_permlane16_swap_b32 v144, v145\n"
      "ds_read_b64_tr_b16 a[6:7], v180 offset:8448\n"
      "v_add_f32 v144, v144, v145\n"
      "ds_read_b64_tr_b16 a[8:9], v178 offset:16384\n"
      "v_mov_b32 v145, v144\n"
      "ds_read_b64_tr_b16 a[10:11], v180 offset:16640\n"
      "s_nop 0\n"
      "v_permlane32_swap_b32 v144, v145\n"
      "ds_read_b64_tr_b16 a[12:13], v178 offset:24576\n"
      "v_add_f32 v144, v144, v145\n"
      "ds_read_b64_tr_b16 a[14:15], v180 offset:24832\n"
      "v_mul_f32 v144, 0x3c000000, v144\n"
      "ds_read_b64_tr_b16 a[16:17], v178 offset:8\n"
      "v_pk_add_f32 v[16:17], v[16:17], v[144:145] op_sel_hi:[1,0] neg_lo:[0,1] neg_hi:[0,1]\n"
      "ds_read_b64_tr_b16 a[18:19], v180 offset:264\n"
      "v_pk_add_f32 v[18:19], v[18:19], v[144:145] op_sel_hi:[1,0] neg_lo:[0,1] neg_hi:[0,1]\n"
      "ds_read_b64_tr_b16 a[20:21], v178 offset:8200\n"
      "v_pk_add_f32 v[20:21], v[20:21], v[144:145] op_sel_hi:[1,0] neg_lo:[0,1] neg_hi:[0,1]\n"
      "ds_read_b64_tr_b16 a[22:23], v180 offset:8456\n"
      "v_cvt_pk_bf16_f32 v48, v16, v17\n"
      "ds_read_b64_tr_b16 a[24:25], v178 offset:16392\n"
      "v_pk_add_f32 v[22:23], v[22:23], v[144:145] op_sel_hi:[1,0] neg_lo:[0,1] neg_hi:[0,1]\n"
      "ds_read_b64_tr_b16 a[26:27], v180 offset:16648\n"
      "v_cvt_pk_bf16_f32 v49, v18, v19\n"
      "ds_read_b64_tr_b16 a[28:29], v178 offset:24584\n"
      "v_pk_add_f32 v[24:25], v[24:25], v[144:145] op_sel_hi:[1,0] neg_lo:[0,1] neg_hi:[0,1]\n"
      "ds_read_b64_tr_b16 a[30:31], v180 offset:24840\n"
      "v_cvt_pk_bf16_f32 v50, v20, v21\n"
      "ds_read_b64_tr_b16 a[32:33], v178 offset:512\n"
      "v_pk_add_f32 v[26:27], v[26:27], v[144:145] op_sel_hi:[1,0] neg_lo:[0,1] neg_hi:[0,1]\n"
      "ds_read_b64_tr_b16 a[34:35], v180 offset:768\n"
      "v_cvt_pk_bf16_f32 v51, v22, v23\n"
      "ds_read_b64_tr_b16 a[36:37], v178 offset:8704\n"
      "v_pk_add_f32 v[28:29], v[28:29], v[144:145] op_sel_hi:[1,0] neg_lo:[0,1] neg_hi:[0,1]\n"
      "ds_read_b64_tr_b16 a[38:39], v180 offset:8960\n"
      "v_cvt_pk_bf16_f32 v52, v24, v25\n"
      "ds_read_b64_tr_b16 a[40:41], v178 offset:16896\n"
      "v_pk_add_f32 v[30:31], v[30:31], v[144:145] op_sel_hi:[1,0] neg_lo:[0,1] neg_hi:[0,1]\n"
      "ds_read_b64_tr_b16 a[42:43], v180 offset:17152\n"
      "v_cvt_pk_bf16_f32 v53, v26, v27\n"
      "ds_read_b64_tr_b16 a[44:45], v178 offset:25088\n"
      "v_pk_add_f32 v[32:33], v[32:33], v[144:145] op_sel_hi:[1,0] neg_lo:[0,1] neg_hi:[0,1]\n"
      "ds_read_b64_tr_b16 a[46:47], v180 offset:25344\n"
      "v_cvt_pk_bf16_f32 v54, v28, v29\n"
      "ds_read_b64_tr_b16 a[48:49], v178 offset:520\n"
      "v_pk_add_f32 v[34:35], v[34:35], v[144:145] op_sel_hi:[1,0] neg_lo:[0,1] neg_hi:[0,1]\n"
      "ds_read_b64_tr_b16 a[50:51], v180 offset:776\n"
      "v_cvt_pk_bf16_f32 v55, v30, v31\n"
      "ds_read_b64_tr_b16 a[52:53], v178 offset:8712\n"
      "v_pk_add_f32 v[36:37], v[36:37], v[144:145] op_sel_hi:[1,0] neg_lo:[0,1] neg_hi:[0,1]\n"
      "ds_read_b64_tr_b16 a[54:55], v180 offset:8968\n"
      "v_cvt_pk_bf16_f32 v56, v32, v33\n"
      "ds_read_b64_tr_b16 a[56:57], v178 offset:16904\n"
      "v_pk_add_f32 v[38:39], v[38:39], v[144:145] op_sel_hi:[1,0] neg_lo:[0,1] neg_hi:[0,1]\n"
      "ds_read_b64_tr_b16 a[58:59], v180 offset:17160\n"
      "v_cvt_pk_bf16_f32 v57, v34, v35\n"
      "ds_read_b64_tr_b16 a[60:61], v178 offset:25096\n"
      "v_pk_add_f32 v[40:41], v[40:41], v[144:145] op_sel_hi:[1,0] neg_lo:[0,1] neg_hi:[0,1]\n"
      "ds_read_b64_tr_b16 a[62:63], v180 offset:25352\n"
      "v_cvt_pk_bf16_f32 v58, v36, v37\n"
      "ds_read_b64_tr_b16 a[64:65], v178 offset:1024\n"
      "v_pk_add_f32 v[42:43], v[42:43], v[144:145] op_sel_hi:[1,0] neg_lo:[0,1] neg_hi:[0,1]\n"
      "ds_read_b64_tr_b16 a[66:67], v180 offset:1280\n"
      "v_cvt_pk_bf16_f32 v59, v38, v39\n"
      "ds_read_b64_tr_b16 a[68:69], v178 offset:9216\n"
      "v_pk_add_f32 v[44:45], v[44:45], v[144:145] op_sel_hi:[1,0] neg_lo:[0,1] neg_hi:[0,1]\n"
      "ds_read_b64_tr_b16 a[70:71], v180 offset:9472\n"
      "v_cvt_pk_bf16_f32 v60, v40, v41\n"
      "ds_read_b64_tr_b16 a[72:73], v178 offset:17408\n"
      "v_pk_add_f32 v[46:47], v[46:47], v[144:145] op_sel_hi:[1,0] neg_lo:[0,1] neg_hi:[0,1]\n"
      "ds_read_b64_tr_b16 a[74:75], v180 offset:17664\n"
      "v_cvt_pk_bf16_f32 v61, v42, v43\n"
      "ds_read_b64_tr_b16 a[76:77], v178 offset:25600\n"
      "v_cvt_pk_bf16_f32 v62, v44, v45\n"
      "ds_read_b64_tr_b16 a[78:79], v180 offset:25856\n"
      "v_cvt_pk_bf16_f32 v63, v46, v47\n"
      "ds_read_b64_tr_b16 a[80:81], v178 offset:1032\n"
      "v_pk_mul_f32 v[144:145], v[16:17], v[16:17]\n"
      "ds_read_b64_tr_b16 a[82:83], v180 offset:1288\n"
      "v_pk_mul_f32 v[146:147], v[18:19], v[18:19]\n"
      "ds_read_b64_tr_b16 a[84:85], v178 offset:9224\n"
      "v_pk_mul_f32 v[148:149], v[20:21], v[20:21]\n"
      "ds_read_b64_tr_b16 a[86:87], v180 offset:9480\n"
      "v_pk_mul_f32 v[150:151], v[22:23], v[22:23]\n"
      "ds_read_b64_tr_b16 a[88:89], v178 offset:17416\n"
      "v_pk_fma_f32 v[144:145], v[24:25], v[24:25], v[144:145]\n"
      "ds_read_b64_tr_b16 a[90:91], v180 offset:17672\n"
      "v_pk_fma_f32 v[146:147], v[26:27], v[26:27], v[146:147]\n"
      "ds_read_b64_tr_b16 a[92:93], v178 offset:25608\n"
      "v_pk_fma_f32 v[148:149], v[28:29], v[28:29], v[148:149]\n"
      "ds_read_b64_tr_b16 a[94:95], v180 offset:25864\n"
      "v_pk_fma_f32 v[150:151], v[30:31], v[30:31], v[150:151]\n"
      "ds_read_b64_tr_b16 a[96:97], v178 offset:1536\n"
      "v_pk_fma_f32 v[144:145], v[32:33], v[32:33], v[144:145]\n"
      "ds_read_b64_tr_b16 a[98:99], v180 offset:1792\n"
      "v_pk_fma_f32 v[146:147], v[34:35], v[34:35], v[146:147]\n"
      "ds_read_b64_tr_b16 a[100:101], v178 offset:9728\n"
      "v_pk_fma_f32 v[148:149], v[36:37], v[36:37], v[148:149]\n"
      "ds_read_b64_tr_b16 a[102:103], v180 offset:9984\n"
      "v_pk_fma_f32 v[150:151], v[38:39], v[38:39], v[150:151]\n"
      "ds_read_b64_tr_b16 a[104:105], v178 offset:17920\n"
      "v_pk_fma_f32 v[144:145], v[40:41], v[40:41], v[144:145]\n"
      "ds_read_b64_tr_b16 a[106:107], v180 offset:18176\n"
      "v_pk_fma_f32 v[146:147], v[42:43], v[42:43], v[146:147]\n"
      "ds_read_b64_tr_b16 a[108:109], v178 offset:26112\n"
      "v_pk_fma_f32 v[148:149], v[44:45], v[44:45], v[148:149]\n"
      "ds_read_b64_tr_b16 a[110:111], v180 offset:26368\n"
      "v_pk_fma_f32 v[150:151], v[46:47], v[46:47], v[150:151]\n"
      "ds_read_b64_tr_b16 a[112:113], v178 offset:1544\n"
      "v_pk_add_f32 v[144:145], v[144:145], v[146:147]\n"
      "ds_read_b64_tr_b16 a[114:115], v180 offset:1800\n"
      "v_pk_add_f32 v[148:149], v[148:149], v[150:151]\n"
      "ds_read_b64_tr_b16 a[116:117], v178 offset:9736\n"
      "v_cndmask_b32 v152, 0, v47, vcc\n"
      "ds_read_b64_tr_b16 a[118:119], v180 offset:9992\n"
      "v_pk_add_f32 v[144:145], v[144:145], v[148:149]\n"
      "ds_read_b64_tr_b16 a[120:121], v178 offset:17928\n"
      "v_mov_b32 v154, 0x3727c5ac\n"
      "ds_read_b64_tr_b16 a[122:123], v180 offset:18184\n"
      "v_add_f32 v144, v144, v145\n"
      "ds_read_b64_tr_b16 a[124:125], v178 offset:26120\n"
      "ds_read_b64_tr_b16 a[126:127], v180 offset:26376\n"
      "s_waitcnt vmcnt(1)\n"
      "s_barrier\n"
      "s_waitcnt lgkmcnt(8)\n"
      "v_mfma_f32_16x16x32_bf16 v[64:67], a[0:3], v[48:51], 0\n"
      "ds_read_b128 a[128:131], v179 offset:32768\n"
      "v_mov_b32 v153, v152\n"
      "v_mfma_f32_16x16x32_bf16 v[64:67], a[4:7], v[52:55], v[64:67]\n"
      "ds_read_b128 a[144:147], v181 offset:33024\n"
      "v_mov_b32 v145, v144\n"
      "v_mfma_f32_16x16x32_bf16 v[64:67], a[8:11], v[56:59], v[64:67]\n"
      "ds_read_b128 a[160:163], v179 offset:40960\n"
      "v_permlane16_swap_b32 v152, v153\n"
      "v_mfma_f32_16x16x32_bf16 v[64:67], a[12:15], v[60:63], v[64:67]\n"
      "ds_read_b128 a[176:179], v181 offset:41216\n"
      "v_permlane16_swap_b32 v144, v145\n"
      "v_mfma_f32_16x16x32_bf16 v[68:71], a[16:19], v[48:51], 0\n"
      "ds_read_b128 a[192:195], v179 offset:49152\n"
      "v_add_f32 v152, v152, v153\n"
      "v_mfma_f32_16x16x32_bf16 v[68:71], a[20:23], v[52:55], v[68:71]\n"
      "ds_read_b128 a[208:211], v181 offset:49408\n"
      "v_add_f32 v144, v144, v145\n"
      "v_mfma_f32_16x16x32_bf16 v[68:71], a[24:27], v[56:59], v[68:71]\n"
      "ds_read_b128 a[224:227], v179 offset:57344\n"
      "v_mov_b32 v153, v152\n"
      "v_mfma_f32_16x16x32_bf16 v[68:71], a[28:31], v[60:63], v[68:71]\n"
      "ds_read_b128 a[240:243], v181 offset:57600\n"
      "v_cvt_pk_bf16_f32 v96, v64, v65\n"
      "v_mfma_f32_16x16x32_bf16 v[72:75], a[32:35], v[48:51], 0\n"
      "ds_read_b128 a[132:135], v179 offset:33280\n"
      "v_cvt_pk_bf16_f32 v97, v66, v67\n"
      "v_mfma_f32_16x16x32_bf16 v[72:75], a[36:39], v[52:55], v[72:75]\n"
      "ds_read_b128 a[148:151], v181 offset:33536\n"
      "v_mov_b32 v145, v144\n"
      "v_mfma_f32_16x16x32_bf16 v[72:75], a[40:43], v[56:59], v[72:75]\n"
      "ds_read_b128 a[164:167], v179 offset:41472\n"
      "v_permlane32_swap_b32 v152, v153\n"
      "v_mfma_f32_16x16x32_bf16 v[72:75], a[44:47], v[60:63], v[72:75]\n"
      "ds_read_b128 a[180:183], v181 offset:41728\n"
      "v_permlane32_swap_b32 v144, v145\n"
      "v_mfma_f32_16x16x32_bf16 v[76:79], a[48:51], v[48:51], 0\n"
      "ds_read_b128 a[196:199], v179 offset:49664\n"
      "v_cvt_pk_bf16_f32 v98, v68, v69\n"
      "v_mfma_f32_16x16x32_bf16 v[76:79], a[52:55], v[52:55], v[76:79]\n"
      "ds_read_b128 a[212:215], v181 offset:49920\n"
      "v_cvt_pk_bf16_f32 v99, v70, v71\n"
      "v_mfma_f32_16x16x32_bf16 v[76:79], a[56:59], v[56:59], v[76:79]\n"
      "ds_read_b128 a[228:231], v179 offset:57856\n"
      "v_add_f32 v152, v152, v153\n"
      "v_mfma_f32_16x16x32_bf16 v[76:79], a[60:63], v[60:63], v[76:79]\n"
      "ds_read_b128 a[244:247], v181 offset:58112\n"
      "v_add_f32 v144, v144, v145\n"
      "v_mfma_f32_16x16x32_bf16 v[80:83], a[64:67], v[48:51], 0\n"
      "ds_read_b128 a[136:139], v179 offset:33792\n"
      "v_fmac_f32 v154, 0x3c000000, v144\n"
      "v_mfma_f32_16x16x32_bf16 v[80:83], a[68:71], v[52:55], v[80:83]\n"
      "ds_read_b128 a[140:143], v179 offset:34304\n"
      "v_cvt_pk_bf16_f32 v100, v72, v73\n"
      "v_mfma_f32_16x16x32_bf16 v[80:83], a[72:75], v[56:59], v[80:83]\n"
      "ds_read_b128 a[152:155], v181 offset:34048\n"
      "v_cvt_pk_bf16_f32 v101, v74, v75\n"
      "v_mfma_f32_16x16x32_bf16 v[80:83], a[76:79], v[60:63], v[80:83]\n"
      "ds_read_b128 a[156:159], v181 offset:34560\n"
      "v_mul_f32 v152, 0x3dc69214, v152\n"
      "v_mfma_f32_16x16x32_bf16 v[84:87], a[80:83], v[48:51], 0\n"
      "ds_read_b128 a[168:171], v179 offset:41984\n"
      "v_rsq_f32 v154, v154\n"
      "v_mfma_f32_16x16x32_bf16 v[84:87], a[84:87], v[52:55], v[84:87]\n"
      "ds_read_b128 a[172:175], v179 offset:42496\n"
      "v_mul_f32 v152, v152, v154\n"
      "v_mfma_f32_16x16x32_bf16 v[84:87], a[88:91], v[56:59], v[84:87]\n"
      "ds_read_b128 a[184:187], v181 offset:42240\n"
      "v_cvt_pk_bf16_f32 v102, v76, v77\n"
      "v_mfma_f32_16x16x32_bf16 v[84:87], a[92:95], v[60:63], v[84:87]\n"
      "ds_read_b128 a[188:191], v181 offset:42752\n"
      "v_cvt_pk_bf16_f32 v103, v78, v79\n"
      "v_mfma_f32_16x16x32_bf16 v[88:91], a[96:99], v[48:51], 0\n"
      "ds_read_b128 a[200:203], v179 offset:50176\n"
      "v_fma_f32 v16, v16, v154, v152\n"
      "v_mfma_f32_16x16x32_bf16 v[88:91], a[100:103], v[52:55], v[88:91]\n"
      "ds_read_b128 a[204:207], v179 offset:50688\n"
      "v_fma_f32 v17, v17, v154, v152\n"
      "v_mfma_f32_16x16x32_bf16 v[88:91], a[104:107], v[56:59], v[88:91]\n"
      "ds_read_b128 a[216:219], v181 offset:50432\n"
      "v_cvt_pk_bf16_f32 v104, v80, v81\n"
      "v_mfma_f32_16x16x32_bf16 v[88:91], a[108:111], v[60:63], v[88:91]\n"
      "ds_read_b128 a[220:223], v181 offset:50944\n"
      "v_cvt_pk_bf16_f32 v105, v82, v83\n"
      "s_waitcnt lgkmcnt(6)\n"
      "v_mfma_f32_16x16x32_bf16 v[92:95], a[112:115], v[48:51], 0\n"
      "ds_read_b128 a[232:235], v179 offset:58368\n"
      "v_cvt_pk_bf16_f32 v0, v16, v17\n"
      "v_mfma_f32_16x16x32_bf16 v[92:95], a[116:119], v[52:55], v[92:95]\n"
      "ds_read_b128 a[236:239], v179 offset:58880\n"
      "v_fma_f32 v18, v18, v154, v152\n"
      "v_mfma_f32_16x16x32_bf16 v[92:95], a[120:123], v[56:59], v[92:95]\n"
      "ds_read_b128 a[248:251], v181 offset:58624\n"
      "v_cvt_pk_bf16_f32 v106, v84, v85\n"
      "v_mfma_f32_16x16x32_bf16 v[92:95], a[124:127], v[60:63], v[92:95]\n"
      "ds_read_b128 a[252:255], v181 offset:59136\n"
      "v_cvt_pk_bf16_f32 v107, v86, v87\n"
      "v_mfma_f32_16x16x32_bf16 v[112:115], a[128:131], v[96:99], 0\n"
      "v_fma_f32 v19, v19, v154, v152\n"
      "v_cvt_pk_bf16_f32 v1, v18, v19\n"
      "v_mfma_f32_16x16x32_bf16 v[116:119], a[144:147], v[96:99], 0\n"
      "v_cvt_pk_bf16_f32 v108, v88, v89\n"
      "v_cvt_pk_bf16_f32 v109, v90, v91\n"
      "v_mfma_f32_16x16x32_bf16 v[120:123], a[160:163], v[96:99], 0\n"
      "v_fma_f32 v20, v20, v154, v152\n"
      "v_fma_f32 v21, v21, v154, v152\n"
      "v_mfma_f32_16x16x32_bf16 v[124:127], a[176:179], v[96:99], 0\n"
      "v_cvt_pk_bf16_f32 v110, v92, v93\n"
      "v_cvt_pk_bf16_f32 v111, v94, v95\n"
      "v_mfma_f32_16x16x32_bf16 v[128:131], a[192:195], v[96:99], 0\n"
      "v_cvt_pk_bf16_f32 v2, v20, v21\n"
      "v_fma_f32 v22, v22, v154, v152\n"
      "v_mfma_f32_16x16x32_bf16 v[132:135], a[208:211], v[96:99], 0\n"
      "v_fma_f32 v23, v23, v154, v152\n"
      "v_cvt_pk_bf16_f32 v3, v22, v23\n"
      "v_mfma_f32_16x16x32_bf16 v[136:139], a[224:227], v[96:99], 0\n"
      "buffer_store_dwordx4 v[0:3], v182, s[52:55], s66 offen offset:0 sc1\n"
      "v_fma_f32 v24, v24, v154, v152\n"
      "v_mfma_f32_16x16x32_bf16 v[140:143], a[240:243], v[96:99], 0\n"
      "v_fma_f32 v25, v25, v154, v152\n"
      "v_cvt_pk_bf16_f32 v4, v24, v25\n"
      "v_mfma_f32_16x16x32_bf16 v[112:115], a[132:135], v[100:103], v[112:115]\n"
      "v_fma_f32 v26, v26, v154, v152\n"
      "v_fma_f32 v27, v27, v154, v152\n"
      "v_mfma_f32_16x16x32_bf16 v[116:119], a[148:151], v[100:103], v[116:119]\n"
      "v_cvt_pk_bf16_f32 v5, v26, v27\n"
      "v_fma_f32 v28, v28, v154, v152\n"
      "v_mfma_f32_16x16x32_bf16 v[120:123], a[164:167], v[100:103], v[120:123]\n"
      "v_fma_f32 v29, v29, v154, v152\n"
      "v_cvt_pk_bf16_f32 v6, v28, v29\n"
      "v_mfma_f32_16x16x32_bf16 v[124:127], a[180:183], v[100:103], v[124:127]\n"
      "v_fma_f32 v30, v30, v154, v152\n"
      "v_fma_f32 v31, v31, v154, v152\n"
      "v_mfma_f32_16x16x32_bf16 v[128:131], a[196:199], v[100:103], v[128:131]\n"
      "v_cvt_pk_bf16_f32 v7, v30, v31\n"
      "buffer_store_dwordx4 v[4:7], v182, s[52:55], s66 offen offset:64 sc1\n"
      "v_mfma_f32_16x16x32_bf16 v[132:135], a[212:215], v[100:103], v[132:135]\n"
      "v_fma_f32 v32, v32, v154, v152\n"
      "v_fma_f32 v33, v33, v154, v152\n"
      "v_mfma_f32_16x16x32_bf16 v[136:139], a[228:231], v[100:103], v[136:139]\n"
      "v_cvt_pk_bf16_f32 v8, v32, v33\n"
      "v_fma_f32 v34, v34, v154, v152\n"
      "v_mfma_f32_16x16x32_bf16 v[140:143], a[244:247], v[100:103], v[140:143]\n"
      "v_fma_f32 v35, v35, v154, v152\n"
      "v_cvt_pk_bf16_f32 v9, v34, v35\n"
      "v_mfma_f32_16x16x32_bf16 v[112:115], a[136:139], v[104:107], v[112:115]\n"
      "v_fma_f32 v36, v36, v154, v152\n"
      "v_fma_f32 v37, v37, v154, v152\n"
      "v_mfma_f32_16x16x32_bf16 v[112:115], a[140:143], v[108:111], v[112:115]\n"
      "v_cvt_pk_bf16_f32 v10, v36, v37\n"
      "v_fma_f32 v38, v38, v154, v152\n"
      "v_mfma_f32_16x16x32_bf16 v[116:119], a[152:155], v[104:107], v[116:119]\n"
      "v_fma_f32 v39, v39, v154, v152\n"
      "v_cvt_pk_bf16_f32 v11, v38, v39\n"
      "v_mfma_f32_16x16x32_bf16 v[116:119], a[156:159], v[108:111], v[116:119]\n"
      "buffer_store_dwordx4 v[8:11], v182, s[52:55], s66 offen offset:128 sc1\n"
      "v_fma_f32 v40, v40, v154, v152\n"
      "v_mfma_f32_16x16x32_bf16 v[120:123], a[168:171], v[104:107], v[120:123]\n"
      "v_fma_f32 v41, v41, v154, v152\n"
      "v_cvt_pk_bf16_f32 v12, v40, v41\n"
      "v_mfma_f32_16x16x32_bf16 v[120:123], a[172:175], v[108:111], v[120:123]\n"
      "v_fma_f32 v42, v42, v154, v152\n"
      "v_fma_f32 v43, v43, v154, v152\n"
      "s_waitcnt lgkmcnt(0)\n"
      "v_mfma_f32_16x16x32_bf16 v[124:127], a[184:187], v[104:107], v[124:127]\n"
      "v_cvt_pk_bf16_f32 v13, v42, v43\n"
      "v_fma_f32 v44, v44, v154, v152\n"
      "v_mfma_f32_16x16x32_bf16 v[124:127], a[188:191], v[108:111], v[124:127]\n"
      "v_fma_f32 v45, v45, v154, v152\n"
      "v_cvt_pk_bf16_f32 v14, v44, v45\n"
      "v_mfma_f32_16x16x32_bf16 v[128:131], a[200:203], v[104:107], v[128:131]\n"
      "v_fma_f32 v46, v46, v154, v152\n"
      "v_fma_f32 v47, v47, v154, v152\n"
      "v_mfma_f32_16x16x32_bf16 v[128:131], a[204:207], v[108:111], v[128:131]\n"
      "v_cndmask_b32_e64 v47, v47, 1.0, vcc\n"
      "v_cvt_pk_bf16_f32 v15, v46, v47\n"
      "v_mfma_f32_16x16x32_bf16 v[132:135], a[216:219], v[104:107], v[132:135]\n"
      "buffer_store_dwordx4 v[12:15], v182, s[52:55], s66 offen offset:192 sc1\n"
      "v_mul_f32 v160, v112, v112\n"
      "v_mfma_f32_16x16x32_bf16 v[132:135], a[220:223], v[108:111], v[132:135]\n"
      "v_mul_f32 v161, v113, v113\n"
      "v_mul_f32 v162, v114, v114\n"
      "v_mfma_f32_16x16x32_bf16 v[136:139], a[232:235], v[104:107], v[136:139]\n"
      "v_mul_f32 v163, v115, v115\n"
      "v_add_f32 v156, v112, v116\n"
      "v_mfma_f32_16x16x32_bf16 v[136:139], a[236:239], v[108:111], v[136:139]\n"
      "v_fmac_f32 v160, v116, v116\n"
      "v_add_f32 v157, v113, v117\n"
      "v_mfma_f32_16x16x32_bf16 v[140:143], a[248:251], v[104:107], v[140:143]\n"
      "v_fmac_f32 v161, v117, v117\n"
      "v_add_f32 v158, v114, v118\n"
      "v_mfma_f32_16x16x32_bf16 v[140:143], a[252:255], v[108:111], v[140:143]\n"
      "v_fmac_f32 v162, v118, v118\n"
      "v_add_f32 v159, v115, v119\n"
      "v_fmac_f32 v163, v119, v119\n"
      "v_add_f32 v156, v156, v120\n"
      "v_fmac_f32 v160, v120, v120\n"
      "v_add_f32 v157, v157, v121\n"
      "v_fmac_f32 v161, v121, v121\n"
      "v_add_f32 v158, v158, v122\n"
      "v_fmac_f32 v162, v122, v122\n"
      "v_add_f32 v159, v159, v123\n"
      "v_fmac_f32 v163, v123, v123\n"
      "v_add_f32 v156, v156, v124\n"
      "v_fmac_f32 v160, v124, v124\n"
      "v_add_f32 v157, v157, v125\n"
      "v_fmac_f32 v161, v125, v125\n"
      "v_add_f32 v158, v158, v126\n"
      "v_fmac_f32 v162, v126, v126\n"
      "v_add_f32 v159, v159, v127\n"
      "v_fmac_f32 v163, v127, v127\n"
      "v_pk_add_f32 v[156:157], v[156:157], v[128:129]\n"
      "v_pk_add_f32 v[158:159], v[158:159], v[130:131]\n"
      "v_pk_fma_f32 v[160:161], v[128:129], v[128:129], v[160:161]\n"
      "v_pk_fma_f32 v[162:163], v[130:131], v[130:131], v[162:163]\n"
      "v_pk_add_f32 v[156:157], v[156:157], v[132:133]\n"
      "v_pk_add_f32 v[158:159], v[158:159], v[134:135]\n"
      "v_pk_fma_f32 v[160:161], v[132:133], v[132:133], v[160:161]\n"
      "v_pk_fma_f32 v[162:163], v[134:135], v[134:135], v[162:163]\n"
      "v_pk_add_f32 v[156:157], v[156:157], v[136:137]\n"
      "v_pk_add_f32 v[158:159], v[158:159], v[138:139]\n"
      "v_pk_fma_f32 v[160:161], v[136:137], v[136:137], v[160:161]\n"
      "v_pk_fma_f32 v[162:163], v[138:139], v[138:139], v[162:163]\n"
      "v_pk_add_f32 v[156:157], v[156:157], v[140:141]\n"
      "v_pk_add_f32 v[158:159], v[158:159], v[142:143]\n"
      "v_pk_fma_f32 v[160:161], v[140:141], v[140:141], v[160:161]\n"
      "v_pk_fma_f32 v[162:163], v[142:143], v[142:143], v[162:163]\n"
      "v_pk_add_f32 v[156:157], v[156:157], v[158:159]\n"
      "v_pk_add_f32 v[160:161], v[160:161], v[162:163]\n"
      "v_cndmask_b32 v164, 0, v143, vcc\n"
      "v_add_f32 v156, v156, v157\n"
      "v_add_f32 v160, v160, v161\n"
      "v_mul_f32 v166, 0x3e0293ee, v154\n"
      "v_mul_f32 v156, 0x3db504f3, v156\n"
      "v_sub_f32 v156, v156, v164\n"
      "v_mov_b32 v157, v156\n"
      "v_mov_b32 v161, v160\n"
      "s_nop 0\n"
      "v_permlane16_swap_b32 v156, v157\n"
      "v_permlane16_swap_b32 v160, v161\n"
      "v_add_f32 v156, v156, v157\n"
      "v_add_f32 v160, v160, v161\n"
      "v_mov_b32 v157, v156\n"
      "v_mov_b32 v161, v160\n"
      "s_nop 0\n"
      "v_permlane32_swap_b32 v156, v157\n"
      "v_permlane32_swap_b32 v160, v161\n"
      "v_add_f32 v156, v156, v157\n"
      "v_add_f32 v160, v160, v161\n"
      "v_mul_f32 v156, 0x3dc69214, v156\n"
      "v_sqrt_f32 v160, v160\n"
      "v_mul_f32 v156, v156, v166\n"
      "v_pk_fma_f32 v[112:113], v[112:113], v[166:167], v[156:157] op_sel_hi:[1,0,0] neg_lo:[0,0,1] neg_hi:[0,0,1]\n"
      "v_pk_fma_f32 v[114:115], v[114:115], v[166:167], v[156:157] op_sel_hi:[1,0,0] neg_lo:[0,0,1] neg_hi:[0,0,1]\n"
      "v_pk_fma_f32 v[116:117], v[116:117], v[166:167], v[156:157] op_sel_hi:[1,0,0] neg_lo:[0,0,1] neg_hi:[0,0,1]\n"
      "v_cvt_pk_bf16_f32 v48, v112, v113\n"
      "v_pk_fma_f32 v[118:119], v[118:119], v[166:167], v[156:157] op_sel_hi:[1,0,0] neg_lo:[0,0,1] neg_hi:[0,0,1]\n"
      "v_cvt_pk_bf16_f32 v49, v114, v115\n"
      "v_pk_fma_f32 v[120:121], v[120:121], v[166:167], v[156:157] op_sel_hi:[1,0,0] neg_lo:[0,0,1] neg_hi:[0,0,1]\n"
      "v_cvt_pk_bf16_f32 v50, v116, v117\n"
      "v_pk_fma_f32 v[122:123], v[122:123], v[166:167], v[156:157] op_sel_hi:[1,0,0] neg_lo:[0,0,1] neg_hi:[0,0,1]\n"
      "v_cvt_pk_bf16_f32 v51, v118, v119\n"
      "v_pk_fma_f32 v[124:125], v[124:125], v[166:167], v[156:157] op_sel_hi:[1,0,0] neg_lo:[0,0,1] neg_hi:[0,0,1]\n"
      "buffer_store_dwordx4 v[48:51], v175, s[56:59], s67 offen offset:0 sc1\n"
      "v_pk_fma_f32 v[126:127], v[126:127], v[166:167], v[156:157] op_sel_hi:[1,0,0] neg_lo:[0,0,1] neg_hi:[0,0,1]\n"
      "v_cvt_pk_bf16_f32 v52, v120, v121\n"
      "v_pk_fma_f32 v[128:129], v[128:129], v[166:167], v[156:157] op_sel_hi:[1,0,0] neg_lo:[0,0,1] neg_hi:[0,0,1]\n"
      "v_cvt_pk_bf16_f32 v53, v122, v123\n"
      "v_pk_fma_f32 v[130:131], v[130:131], v[166:167], v[156:157] op_sel_hi:[1,0,0] neg_lo:[0,0,1] neg_hi:[0,0,1]\n"
      "v_cvt_pk_bf16_f32 v54, v124, v125\n"
      "v_pk_fma_f32 v[132:133], v[132:133], v[166:167], v[156:157] op_sel_hi:[1,0,0] neg_lo:[0,0,1] neg_hi:[0,0,1]\n"
      "v_cvt_pk_bf16_f32 v55, v126, v127\n"
      "v_pk_fma_f32 v[134:135], v[134:135], v[166:167], v[156:157] op_sel_hi:[1,0,0] neg_lo:[0,0,1] neg_hi:[0,0,1]\n"
      "buffer_store_dwordx4 v[52:55], v175, s[56:59], s67 offen offset:1024 sc1\n"
      "v_pk_fma_f32 v[136:137], v[136:137], v[166:167], v[156:157] op_sel_hi:[1,0,0] neg_lo:[0,0,1] neg_hi:[0,0,1]\n"
      "v_cvt_pk_bf16_f32 v56, v128, v129\n"
      "v_pk_fma_f32 v[138:139], v[138:139], v[166:167], v[156:157] op_sel_hi:[1,0,0] neg_lo:[0,0,1] neg_hi:[0,0,1]\n"
      "v_cvt_pk_bf16_f32 v57, v130, v131\n"
      "v_pk_fma_f32 v[140:141], v[140:141], v[166:167], v[156:157] op_sel_hi:[1,0,0] neg_lo:[0,0,1] neg_hi:[0,0,1]\n"
      "v_cvt_pk_bf16_f32 v58, v132, v133\n"
      "v_pk_fma_f32 v[142:143], v[142:143], v[166:167], v[156:157] op_sel_hi:[1,0,0] neg_lo:[0,0,1] neg_hi:[0,0,1]\n"
      "v_cvt_pk_bf16_f32 v59, v134, v135\n"
      "v_mul_f32 v160, v160, v166\n"
      "v_mov_b32 v164, 0xc2c80000\n"
      "v_fmac_f32 v164, 0x41366666, v160\n"
      "v_max_f32 v164, 0, v164\n"
      "v_add_u32 v164, 0xffff, v164\n"
      "v_and_b32 v164, 0xffff0000, v164\n"
      "v_xor_b32 v164, 0x80000000, v164\n"
      "buffer_store_dwordx4 v[56:59], v175, s[56:59], s67 offen offset:2048 sc1\n"
      "v_cvt_pk_bf16_f32 v60, v136, v137\n"
      "v_cvt_pk_bf16_f32 v61, v138, v139\n"
      "v_cvt_pk_bf16_f32 v62, v140, v141\n"
      "v_cndmask_b32 v143, v143, v164, vcc\n"
      "v_cvt_pk_bf16_f32 v63, v142, v143\n"
      "buffer_store_dwordx4 v[60:63], v175, s[56:59], s67 offen offset:3072 sc1\n"
      "s_endpgm\n"
      "2:\n"
      "s_load_dwordx2 s[72:73], %[ka], 56\n"
      "s_cmp_ge_u32 %[b], 58\n"
      "s_cselect_b32 s67, 24, 48\n"
      "s_load_dwordx2 s[76:77], %[ka], s67\n"
      "v_readfirstlane_b32 s64, %[t]\n"
      "v_lshlrev_b32 v170, 6, %[t]\n"
      "v_and_b32 v171, 0x700, v170\n"
      "v_lshlrev_b32 v172, 1, %[t]\n"
      "v_and_or_b32 v171, v172, 64, v171\n"
      "v_lshlrev_b32 v175, 4, %[t]\n"
      "v_and_b32 v173, 16, %[t]\n"
      "v_and_b32 v174, 48, v175\n"
      "s_lshr_b32 s64, s64, 6\n"
      "s_lshl_b32 s65, s64, 13\n"
      "v_xor_b32 v173, v173, v174\n"
      "v_add3_u32 v176, v171, v173, s65\n"
      "v_xor_b32 v177, 32, v176\n"
      "s_movk_i32 s66, 0x1000\n"
      "v_and_b32 v175, 0x3f0, v175\n"
      "s_mov_b32 s74, 32768\n"
      "s_mov_b32 s75, 0x20000\n"
      "s_mov_b32 s78, 32768\n"
      "s_mov_b32 s79, 0x20000\n"
      "s_mov_b32 s58, 33554432\n"
      "s_mov_b32 s59, 0x20000\n"
      "s_add_u32 m0, s65, 0\n"
      "s_waitcnt lgkmcnt(0)\n"
      "buffer_load_dwordx4 v176, s[72:75], 0 offen offset:0 lds\n"
      "buffer_load_dwordx4 v177, s[72:75], 0 offen offset:2048 lds\n"
      "s_add_u32 m0, s65, 896\n"
      "s_nop 0\n"
      "buffer_load_dwordx4 v176, s[72:75], 0 offen offset:128 lds\n"
      "buffer_load_dwordx4 v177, s[72:75], 0 offen offset:2176 lds\n"
      "s_add_u32 m0, s65, 4096\n"
      "s_nop 0\n"
      "buffer_load_dwordx4 v176, s[72:75], s66 offen offset:0 lds\n"
      "buffer_load_dwordx4 v177, s[72:75], s66 offen offset:2048 lds\n"
      "s_add_u32 m0, s65, 4992\n"
      "s_nop 0\n"
      "buffer_load_dwordx4 v176, s[72:75], s66 offen offset:128 lds\n"
      "buffer_load_dwordx4 v177, s[72:75], s66 offen offset:2176 lds\n"
      "s_add_u32 m0, s65, 32768\n"
      "s_nop 0\n"
      "buffer_load_dwordx4 v176, s[76:79], 0 offen offset:0 lds\n"
      "buffer_load_dwordx4 v177, s[76:79], 0 offen offset:2048 lds\n"
      "s_add_u32 m0, s65, 33664\n"
      "s_nop 0\n"
      "buffer_load_dwordx4 v176, s[76:79], 0 offen offset:128 lds\n"
      "buffer_load_dwordx4 v177, s[76:79], 0 offen offset:2176 lds\n"
      "s_add_u32 m0, s65, 36864\n"
      "s_nop 0\n"
      "buffer_load_dwordx4 v176, s[76:79], s66 offen offset:0 lds\n"
      "buffer_load_dwordx4 v177, s[76:79], s66 offen offset:2048 lds\n"
      "s_add_u32 m0, s65, 37760\n"
      "s_nop 0\n"
      "buffer_load_dwordx4 v176, s[76:79], s66 offen offset:128 lds\n"
      "buffer_load_dwordx4 v177, s[76:79], s66 offen offset:2176 lds\n"
      "s_sub_u32 s69, %[b], 56\n"
      "s_lshl_b32 s68, s69, 2\n"
      "s_add_u32 s68, s68, s64\n"
      "v_mov_b32 v0, 0\n"
      "v_mov_b32 v1, 0\n"
      "v_mov_b32 v2, 0\n"
      "v_mov_b32 v3, 0\n"
      "s_lshl_b32 s69, s69, 8\n"
      "v_add_u32 v4, s69, %[t]\n"
      "v_cmp_gt_u32 vcc, 704, v4\n"
      "v_lshlrev_b32 v5, 4, v4\n"
      "v_mov_b32 v6, 0x7fff0000\n"
      "v_cndmask_b32 v5, v6, v5, vcc\n"
      "s_mov_b32 s69, 3204096\n"
      "buffer_store_dwordx4 v[0:3], v5, s[56:59], s69 offen sc1\n"
      "v_lshlrev_b32 v178, 7, %[t]\n"
      "v_and_b32 v178, 0x1800, v178\n"
      "v_and_b32 v179, 0xf0, v175\n"
      "v_and_b32 v172, 32, v172\n"
      "v_xor_b32 v179, v179, v172\n"
      "v_or_b32 v178, v178, v179\n"
      "v_xor_b32 v180, 16, v178\n"
      "s_and_b32 s69, s68, 6\n"
      "s_lshl_b32 s69, s69, 8\n"
      "s_and_b32 s70, s68, 1\n"
      "s_lshl_b32 s70, s70, 3\n"
      "s_add_u32 s69, s69, s70\n"
      "v_add_u32 v178, s69, v178\n"
      "v_add_u32 v180, s69, v180\n"
      "v_lshlrev_b32 v179, 8, %[t]\n"
      "v_and_b32 v179, 0x800, v179\n"
      "v_and_b32 v170, 0x1c0, v170\n"
      "v_lshlrev_b32 v171, 2, %[t]\n"
      "v_xor_b32 v171, v171, %[t]\n"
      "v_and_b32 v171, 48, v171\n"
      "v_or3_b32 v179, v179, v170, v171\n"
      "s_mov_b32 vcc_lo, 0\n"
      "s_mov_b32 vcc_hi, 0xffff0000\n"
      "s_lshl_b32 s68, s68, 12\n"
      "s_add_u32 s68, s68, 3215360\n"
      "v_subrev_u32 v176, s65, v176\n"
      "v_subrev_u32 v177, s65, v177\n"
      "s_waitcnt vmcnt(1)\n"
      "s_barrier\n"
      "ds_read_b64_tr_b16 v[8:9], v178 offset:0\n"
      "ds_read_b64_tr_b16 v[10:11], v180 offset:256\n"
      "ds_read_b64_tr_b16 v[12:13], v178 offset:8192\n"
      "ds_read_b64_tr_b16 v[14:15], v180 offset:8448\n"
      "ds_read_b64_tr_b16 v[16:17], v178 offset:16384\n"
      "ds_read_b64_tr_b16 v[18:19], v180 offset:16640\n"
      "ds_read_b64_tr_b16 v[20:21], v178 offset:24576\n"
      "ds_read_b64_tr_b16 v[22:23], v180 offset:24832\n"
      "ds_read_b128 a[0:3], v179 offset:32768\n"
      "ds_read_b128 a[4:7], v179 offset:33280\n"
      "ds_read_b128 a[8:11], v179 offset:33792\n"
      "ds_read_b128 a[12:15], v179 offset:34304\n"
      "ds_read_b128 a[16:19], v179 offset:36864\n"
      "ds_read_b128 a[20:23], v179 offset:37376\n"
      "ds_read_b128 a[24:27], v179 offset:37888\n"
      "ds_read_b128 a[28:31], v179 offset:38400\n"
      "s_waitcnt lgkmcnt(7)\n"
      "v_mfma_f32_16x16x32_bf16 v[32:35], a[0:3], v[8:11], 0\n"
      "ds_read_b128 a[32:35], v179 offset:40960\n"
      "s_waitcnt lgkmcnt(7)\n"
      "v_mfma_f32_16x16x32_bf16 v[32:35], a[4:7], v[12:15], v[32:35]\n"
      "ds_read_b128 a[36:39], v179 offset:41472\n"
      "s_waitcnt lgkmcnt(7)\n"
      "v_mfma_f32_16x16x32_bf16 v[32:35], a[8:11], v[16:19], v[32:35]\n"
      "ds_read_b128 a[40:43], v179 offset:41984\n"
      "s_waitcnt lgkmcnt(7)\n"
      "v_mfma_f32_16x16x32_bf16 v[32:35], a[12:15], v[20:23], v[32:35]\n"
      "ds_read_b128 a[44:47], v179 offset:42496\n"
      "s_waitcnt lgkmcnt(7)\n"
      "v_mfma_f32_16x16x32_bf16 v[36:39], a[16:19], v[8:11], 0\n"
      "ds_read_b128 a[48:51], v179 offset:45056\n"
      "s_waitcnt lgkmcnt(7)\n"
      "v_mfma_f32_16x16x32_bf16 v[36:39], a[20:23], v[12:15], v[36:39]\n"
      "ds_read_b128 a[52:55], v179 offset:45568\n"
      "s_waitcnt lgkmcnt(6)\n"
      "v_mfma_f32_16x16x32_bf16 v[36:39], a[24:27], v[16:19], v[36:39]\n"
      "ds_read_b128 a[56:59], v179 offset:46080\n"
      "v_mfma_f32_16x16x32_bf16 v[36:39], a[28:31], v[20:23], v[36:39]\n"
      "ds_read_b128 a[60:63], v179 offset:46592\n"
      "s_waitcnt lgkmcnt(6)\n"
      "v_mfma_f32_16x16x32_bf16 v[40:43], a[32:35], v[8:11], 0\n"
      "ds_read_b128 a[64:67], v179 offset:49152\n"
      "v_mfma_f32_16x16x32_bf16 v[40:43], a[36:39], v[12:15], v[40:43]\n"
      "ds_read_b128 a[68:71], v179 offset:49664\n"
      "s_waitcnt lgkmcnt(7)\n"
      "v_mfma_f32_16x16x32_bf16 v[40:43], a[40:43], v[16:19], v[40:43]\n"
      "ds_read_b128 a[72:75], v179 offset:50176\n"
      "s_waitcnt lgkmcnt(7)\n"
      "v_mfma_f32_16x16x32_bf16 v[40:43], a[44:47], v[20:23], v[40:43]\n"
      "ds_read_b128 a[76:79], v179 offset:50688\n"
      "v_add_f32 v24, v32, v36\n"
      "s_waitcnt lgkmcnt(6)\n"
      "v_mfma_f32_16x16x32_bf16 v[44:47], a[48:51], v[8:11], 0\n"
      "ds_read_b128 a[80:83], v179 offset:53248\n"
      "v_add_f32 v25, v33, v37\n"
      "v_mfma_f32_16x16x32_bf16 v[44:47], a[52:55], v[12:15], v[44:47]\n"
      "ds_read_b128 a[84:87], v179 offset:53760\n"
      "v_add_f32 v26, v34, v38\n"
      "s_waitcnt lgkmcnt(6)\n"
      "v_mfma_f32_16x16x32_bf16 v[44:47], a[56:59], v[16:19], v[44:47]\n"
      "ds_read_b128 a[88:91], v179 offset:54272\n"
      "v_add_f32 v27, v35, v39\n"
      "v_mfma_f32_16x16x32_bf16 v[44:47], a[60:63], v[20:23], v[44:47]\n"
      "ds_read_b128 a[92:95], v179 offset:54784\n"
      "v_add_f32 v24, v24, v40\n"
      "s_waitcnt lgkmcnt(5)\n"
      "v_mfma_f32_16x16x32_bf16 v[48:51], a[64:67], v[8:11], 0\n"
      "ds_read_b128 a[96:99], v179 offset:57344\n"
      "v_add_f32 v25, v25, v41\n"
      "v_mfma_f32_16x16x32_bf16 v[48:51], a[68:71], v[12:15], v[48:51]\n"
      "ds_read_b128 a[100:103], v179 offset:57856\n"
      "v_add_f32 v26, v26, v42\n"
      "v_mfma_f32_16x16x32_bf16 v[48:51], a[72:75], v[16:19], v[48:51]\n"
      "ds_read_b128 a[104:107], v179 offset:58368\n"
      "v_add_f32 v27, v27, v43\n"
      "s_waitcnt lgkmcnt(5)\n"
      "v_mfma_f32_16x16x32_bf16 v[48:51], a[76:79], v[20:23], v[48:51]\n"
      "ds_read_b128 a[108:111], v179 offset:58880\n"
      "v_add_f32 v24, v24, v44\n"
      "v_mfma_f32_16x16x32_bf16 v[52:55], a[80:83], v[8:11], 0\n"
      "ds_read_b128 a[112:115], v179 offset:61440\n"
      "v_add_f32 v25, v25, v45\n"
      "v_mfma_f32_16x16x32_bf16 v[52:55], a[84:87], v[12:15], v[52:55]\n"
      "ds_read_b128 a[116:119], v179 offset:61952\n"
      "v_add_f32 v26, v26, v46\n"
      "s_waitcnt lgkmcnt(5)\n"
      "v_mfma_f32_16x16x32_bf16 v[52:55], a[88:91], v[16:19], v[52:55]\n"
      "ds_read_b128 a[120:123], v179 offset:62464\n"
      "v_add_f32 v27, v27, v47\n"
      "v_mfma_f32_16x16x32_bf16 v[52:55], a[92:95], v[20:23], v[52:55]\n"
      "ds_read_b128 a[124:127], v179 offset:62976\n"
      "v_add_f32 v24, v24, v48\n"
      "v_mfma_f32_16x16x32_bf16 v[56:59], a[96:99], v[8:11], 0\n"
      "v_add_f32 v25, v25, v49\n"
      "s_waitcnt lgkmcnt(5)\n"
      "v_mfma_f32_16x16x32_bf16 v[56:59], a[100:103], v[12:15], v[56:59]\n"
      "v_add_f32 v26, v26, v50\n"
      "v_mfma_f32_16x16x32_bf16 v[56:59], a[104:107], v[16:19], v[56:59]\n"
      "v_add_f32 v27, v27, v51\n"
      "s_waitcnt lgkmcnt(3)\n"
      "v_mfma_f32_16x16x32_bf16 v[56:59], a[108:111], v[20:23], v[56:59]\n"
      "v_add_f32 v24, v24, v52\n"
      "v_mfma_f32_16x16x32_bf16 v[60:63], a[112:115], v[8:11], 0\n"
      "v_add_f32 v25, v25, v53\n"
      "s_waitcnt lgkmcnt(2)\n"
      "v_mfma_f32_16x16x32_bf16 v[60:63], a[116:119], v[12:15], v[60:63]\n"
      "v_add_f32 v26, v26, v54\n"
      "s_waitcnt lgkmcnt(1)\n"
      "v_mfma_f32_16x16x32_bf16 v[60:63], a[120:123], v[16:19], v[60:63]\n"
      "v_add_f32 v27, v27, v55\n"
      "s_waitcnt lgkmcnt(0)\n"
      "v_mfma_f32_16x16x32_bf16 v[60:63], a[124:127], v[20:23], v[60:63]\n"
      "v_add_f32 v24, v24, v56\n"
      "v_add_f32 v25, v25, v57\n"
      "v_add_f32 v26, v26, v58\n"
      "v_add_f32 v27, v27, v59\n"
      "s_nop 3\n"
      "v_add_f32 v24, v24, v60\n"
      "v_add_f32 v25, v25, v61\n"
      "v_add_f32 v26, v26, v62\n"
      "v_add_f32 v27, v27, v63\n"
      "v_add_f32 v24, v24, v25\n"
      "v_add_f32 v26, v26, v27\n"
      "v_cndmask_b32 v28, 0, v63, vcc\n"
      "v_add_f32 v24, v24, v26\n"
      "v_mul_f32 v24, 0x3db504f3, v24\n"
      "v_sub_f32 v24, v24, v28\n"
      "v_mov_b32 v25, v24\n"
      "s_nop 1\n"
      "v_permlane16_swap_b32 v24, v25\n"
      "v_add_f32 v24, v24, v25\n"
      "v_mov_b32 v25, v24\n"
      "s_nop 1\n"
      "v_permlane32_swap_b32 v24, v25\n"
      "v_add_f32 v24, v24, v25\n"
      "v_mul_f32 v24, 0x3dc69214, v24\n"
      "v_pk_add_f32 v[32:33], v[32:33], v[24:25] op_sel_hi:[1,0] neg_lo:[0,1] neg_hi:[0,1]\n"
      "v_pk_add_f32 v[34:35], v[34:35], v[24:25] op_sel_hi:[1,0] neg_lo:[0,1] neg_hi:[0,1]\n"
      "v_pk_add_f32 v[36:37], v[36:37], v[24:25] op_sel_hi:[1,0] neg_lo:[0,1] neg_hi:[0,1]\n"
      "v_cvt_pk_bf16_f32 v8, v32, v33\n"
      "v_pk_add_f32 v[38:39], v[38:39], v[24:25] op_sel_hi:[1,0] neg_lo:[0,1] neg_hi:[0,1]\n"
      "v_cvt_pk_bf16_f32 v9, v34, v35\n"
      "v_pk_add_f32 v[40:41], v[40:41], v[24:25] op_sel_hi:[1,0] neg_lo:[0,1] neg_hi:[0,1]\n"
      "v_cvt_pk_bf16_f32 v10, v36, v37\n"
      "v_pk_add_f32 v[42:43], v[42:43], v[24:25] op_sel_hi:[1,0] neg_lo:[0,1] neg_hi:[0,1]\n"
      "v_cvt_pk_bf16_f32 v11, v38, v39\n"
      "v_pk_add_f32 v[44:45], v[44:45], v[24:25] op_sel_hi:[1,0] neg_lo:[0,1] neg_hi:[0,1]\n"
      "buffer_store_dwordx4 v[8:11], v176, s[56:59], s68 offen offset:0 sc1\n"
      "v_pk_add_f32 v[46:47], v[46:47], v[24:25] op_sel_hi:[1,0] neg_lo:[0,1] neg_hi:[0,1]\n"
      "v_cvt_pk_bf16_f32 v12, v40, v41\n"
      "v_pk_add_f32 v[48:49], v[48:49], v[24:25] op_sel_hi:[1,0] neg_lo:[0,1] neg_hi:[0,1]\n"
      "v_cvt_pk_bf16_f32 v13, v42, v43\n"
      "v_pk_add_f32 v[50:51], v[50:51], v[24:25] op_sel_hi:[1,0] neg_lo:[0,1] neg_hi:[0,1]\n"
      "v_cvt_pk_bf16_f32 v14, v44, v45\n"
      "v_pk_add_f32 v[52:53], v[52:53], v[24:25] op_sel_hi:[1,0] neg_lo:[0,1] neg_hi:[0,1]\n"
      "v_cvt_pk_bf16_f32 v15, v46, v47\n"
      "v_pk_add_f32 v[54:55], v[54:55], v[24:25] op_sel_hi:[1,0] neg_lo:[0,1] neg_hi:[0,1]\n"
      "buffer_store_dwordx4 v[12:15], v176, s[56:59], s68 offen offset:128 sc1\n"
      "v_pk_add_f32 v[56:57], v[56:57], v[24:25] op_sel_hi:[1,0] neg_lo:[0,1] neg_hi:[0,1]\n"
      "v_cvt_pk_bf16_f32 v16, v48, v49\n"
      "v_pk_add_f32 v[58:59], v[58:59], v[24:25] op_sel_hi:[1,0] neg_lo:[0,1] neg_hi:[0,1]\n"
      "v_cvt_pk_bf16_f32 v17, v50, v51\n"
      "v_pk_add_f32 v[60:61], v[60:61], v[24:25] op_sel_hi:[1,0] neg_lo:[0,1] neg_hi:[0,1]\n"
      "v_cvt_pk_bf16_f32 v18, v52, v53\n"
      "v_pk_add_f32 v[62:63], v[62:63], v[24:25] op_sel_hi:[1,0] neg_lo:[0,1] neg_hi:[0,1]\n"
      "v_cvt_pk_bf16_f32 v19, v54, v55\n"
      "buffer_store_dwordx4 v[16:19], v177, s[56:59], s68 offen offset:2048 sc1\n"
      "v_cvt_pk_bf16_f32 v20, v56, v57\n"
      "v_cvt_pk_bf16_f32 v21, v58, v59\n"
      "v_cvt_pk_bf16_f32 v22, v60, v61\n"
      "v_cndmask_b32_e64 v63, v63, 0, vcc\n"
      "v_cvt_pk_bf16_f32 v23, v62, v63\n"
      "buffer_store_dwordx4 v[20:23], v177, s[56:59], s68 offen offset:2176 sc1\n"
      "3:\n"
      : [t] "+{v190}"(t)
      : [ka] "s"(__builtin_amdgcn_kernarg_segment_ptr()), [b] "s"(blockIdx.x), "s"(ldsaddr(sm))
      : "v0", "v1", "v2", "v3", "v4", "v5", "v6", "v7", "v8", "v9", "v10", "v11", "v12", "v13", "v14", "v15", "v16", "v17", "v18", "v19", "v20", "v21", "v22", "v23", "v24", "v25", "v26", "v27", "v28", "v29", "v30", "v31", "v32", "v33", "v34", "v35", "v36", "v37", "v38", "v39", "v40", "v41", "v42", "v43", "v44", "v45", "v46", "v47", "v48", "v49", "v50", "v51", "v52", "v53", "v54", "v55", "v56", "v57", "v58", "v59", "v60", "v61", "v62", "v63", "v64", "v65", "v66", "v67", "v68", "v69", "v70", "v71", "v72", "v73", "v74", "v75", "v76", "v77", "v78", "v79", "v80", "v81", "v82", "v83", "v84", "v85", "v86", "v87", "v88", "v89", "v90", "v91", "v92", "v93", "v94", "v95", "v96", "v97", "v98", "v99", "v100", "v101", "v102", "v103", "v104", "v105", "v106", "v107", "v108", "v109", "v110", "v111", "v112", "v113", "v114", "v115", "v116", "v117", "v118", "v119", "v120", "v121", "v122", "v123", "v124", "v125", "v126", "v127", "v128", "v129", "v130", "v131", "v132", "v133", "v134", "v135", "v136", "v137", "v138", "v139", "v140", "v141", "v142", "v143", "v144", "v145", "v146", "v147", "v148", "v149", "v150", "v151", "v152", "v153", "v154", "v155", "v156", "v157", "v158", "v159", "v160", "v161", "v162", "v163", "v164", "v165", "v166", "v167", "v168", "v169", "v170", "v171", "v172", "v173", "v174", "v175", "v176", "v177", "v178", "v179", "v180", "v181", "v182", "v183", "v184", "v185", "v186", "v187", "v188", "v189", "a0", "a1", "a2", "a3", "a4", "a5", "a6", "a7", "a8", "a9", "a10", "a11", "a12", "a13", "a14", "a15", "a16", "a17", "a18", "a19", "a20", "a21", "a22", "a23", "a24", "a25", "a26", "a27", "a28", "a29", "a30", "a31", "a32", "a33", "a34", "a35", "a36", "a37", "a38", "a39", "a40", "a41", "a42", "a43", "a44", "a45", "a46", "a47", "a48", "a49", "a50", "a51", "a52", "a53", "a54", "a55", "a56", "a57", "a58", "a59", "a60", "a61", "a62", "a63", "a64", "a65", "a66", "a67", "a68", "a69", "a70", "a71", "a72", "a73", "a74", "a75", "a76", "a77", "a78", "a79", "a80", "a81", "a82", "a83", "a84", "a85", "a86", "a87", "a88", "a89", "a90", "a91", "a92", "a93", "a94", "a95", "a96", "a97", "a98", "a99", "a100", "a101", "a102", "a103", "a104", "a105", "a106", "a107", "a108", "a109", "a110", "a111", "a112", "a113", "a114", "a115", "a116", "a117", "a118", "a119", "a120", "a121", "a122", "a123", "a124", "a125", "a126", "a127", "a128", "a129", "a130", "a131", "a132", "a133", "a134", "a135", "a136", "a137", "a138", "a139", "a140", "a141", "a142", "a143", "a144", "a145", "a146", "a147", "a148", "a149", "a150", "a151", "a152", "a153", "a154", "a155", "a156", "a157", "a158", "a159", "a160", "a161", "a162", "a163", "a164", "a165", "a166", "a167", "a168", "a169", "a170", "a171", "a172", "a173", "a174", "a175", "a176", "a177", "a178", "a179", "a180", "a181", "a182", "a183", "a184", "a185", "a186", "a187", "a188", "a189", "a190", "a191", "a192", "a193", "a194", "a195", "a196", "a197", "a198", "a199", "a200", "a201", "a202", "a203", "a204", "a205", "a206", "a207", "a208", "a209", "a210", "a211", "a212", "a213", "a214", "a215", "a216", "a217", "a218", "a219", "a220", "a221", "a222", "a223", "a224", "a225", "a226", "a227", "a228", "a229", "a230", "a231", "a232", "a233", "a234", "a235", "a236", "a237", "a238", "a239", "a240", "a241", "a242", "a243", "a244", "a245", "a246", "a247", "a248", "a249", "a250", "a251", "a252", "a253", "a254", "a255", "s40", "s41", "s42", "s43", "s44", "s45", "s46", "s47", "s48", "s49", "s50", "s51", "s52", "s53", "s54", "s55", "s56", "s57", "s58", "s59", "s60", "s61", "s62", "s63", "s64", "s65", "s66", "s67", "s68", "s69", "s70", "s71", "s72", "s73", "s74", "s75", "s76", "s77", "s78", "s79", "s80", "vcc", "scc", "memory");
}

constexpr unsigned L_TILE = 65536, L_LIST = 131072, LIST_B = 4096, L_W3 = L_LIST + 4 * LIST_B;
__global__ __launch_bounds__(256, 1) void k2_attn(const unsigned short* __restrict__ x, const unsigned short* __restrict__ deg_inv,
                                                  char* __restrict__ ws, unsigned short* __restrict__ out) {
  __shared__ __attribute__((aligned(16))) char sm[L_W3 + 16384];
  unsigned t = threadIdx.x, stat;
  f32x4 acc[8];
  asm volatile(
      "s_load_dwordx2 s[40:41], %[ka], 0x10\n"
      "s_load_dwordx2 s[44:45], %[ka], 0x10\n"
      "s_load_dwordx2 s[48:49], %[ka], 0x0\n"
      "s_load_dwordx2 s[52:53], %[ka], 0x18\n"
      "s_load_dwordx2 s[56:57], %[ka], 0x8\n"
      "s_getpc_b64 s[60:61]\n"
      ".Lk2_pc:\n"
      "s_mov_b32 s62, .Lk2_asm_end-.Lk2_pc\n"
      "s_mov_b32 s63, 0x20000\n"
      "v_readfirstlane_b32 s64, %[t]\n"
      "s_lshr_b32 s72, %[b], 3\n"
      "s_mul_i32 s73, s72, 205\n"
      "s_lshr_b32 s73, s73, 10\n"
      "s_mul_i32 s74, s73, 5\n"
      "s_sub_u32 s71, s72, s74\n"
      "s_and_b32 s72, %[b], 7\n"
      "s_mul_i32 s70, s72, 6\n"
      "s_add_u32 s70, s70, s73\n"
      "s_cmp_ge_u32 %[b], 240\n"
      "s_cselect_b32 s70, 48, s70\n"
      "s_cselect_b32 s71, s72, s71\n"
      "s_lshr_b32 s64, s64, 6\n"
      "s_lshl_b32 s65, s64, 12\n"
      "s_mul_i32 s80, s71, 638976\n"
      "v_lshlrev_b32 v0, 6, %[t]\n"
      "v_and_b32 v1, 0x700, v0\n"
      "v_lshlrev_b32 v2, 1, %[t]\n"
      "v_and_or_b32 v1, v2, 64, v1\n"
      "v_lshlrev_b32 v242, 4, %[t]\n"
      "v_and_b32 v3, 16, %[t]\n"
      "v_and_b32 v242, 0x3f0, v242\n"
      "s_add_u32 s72, s65, 0x1000\n"
      "v_and_b32 v4, 48, v242\n"
      "v_xor_b32 v3, v3, v4\n"
      "v_add3_u32 v214, v1, v3, s72\n"
      "v_subrev_u32 v215, 896, v214\n"
      "v_xor_b32 v216, 32, v214\n"
      "v_subrev_u32 v217, 896, v216\n"
      "s_mov_b32 s42, 3280896\n"
      "s_mov_b32 s43, 0x20000\n"
      "s_mov_b32 s46, 0x2000000\n"
      "s_mov_b32 s47, 0x20000\n"
      "s_lshl_b32 s72, s70, 2\n"
      "s_add_u32 s72, s72, s64\n"
      "s_lshl_b32 s76, s72, 14\n"
      "s_add_u32 s76, s76, 4194304\n"
      "s_add_u32 s77, s76, 0x1000\n"
      "s_add_u32 s78, s76, 0x2000\n"
      "s_add_u32 s79, s76, 0x3000\n"
      "s_lshl_b32 s73, s71, 2\n"
      "s_add_u32 s73, s73, s64\n"
      "s_mul_i32 s74, s70, 5\n"
      "s_add_u32 s74, s74, s71\n"
      "s_mul_i32 s74, s74, 256\n"
      "s_add_u32 s74, s74, s64\n"
      "s_lshl_b32 s74, s74, 2\n"
      "s_add_u32 s74, s74, 29360128\n"
      "s_add_u32 s89, s80, 638976\n"
      "s_add_u32 m0, s65, 32768\n"
      "s_waitcnt lgkmcnt(0)\n"
      "buffer_load_dword v228, v242, s[44:47], s74 offen\n"
      "buffer_load_dwordx4 v214, s[40:43], s80 offen offset:0 lds\n"
      "buffer_load_dwordx4 v215, s[40:43], s80 offen offset:1024 lds\n"
      "buffer_load_dwordx4 v216, s[40:43], s80 offen offset:2048 lds\n"
      "buffer_load_dwordx4 v217, s[40:43], s80 offen offset:3072 lds\n"
      "s_add_u32 s80, s80, 0x4000\n"
      "buffer_load_dwordx4 a[0:3], v242, s[44:47], s76 offen offset:0\n"
      "buffer_load_dwordx4 a[4:7], v242, s[44:47], s76 offen offset:1024\n"
      "buffer_load_dwordx4 a[8:11], v242, s[44:47], s76 offen offset:2048\n"
      "buffer_load_dwordx4 a[12:15], v242, s[44:47], s76 offen offset:3072\n"
      "buffer_load_dwordx4 a[16:19], v242, s[44:47], s77 offen offset:0\n"
      "buffer_load_dwordx4 a[20:23], v242, s[44:47], s77 offen offset:1024\n"
      "buffer_load_dwordx4 a[24:27], v242, s[44:47], s77 offen offset:2048\n"
      "buffer_load_dwordx4 a[28:31], v242, s[44:47], s77 offen offset:3072\n"
      "v_lshlrev_b32 v1, 9, %[t]\n"
      "v_and_b32 v1, 0x1800, v1\n"
      "v_and_b32 v0, 0xc0, v0\n"
      "v_lshlrev_b32 v6, 3, %[t]\n"
      "v_and_b32 v3, 32, v6\n"
      "v_and_b32 v7, 48, %[t]\n"
      "v_xor_b32 v3, v3, v7\n"
      "v_or3_b32 v1, v1, v0, v3\n"
      "v_subrev_u32 v240, s65, v1\n"
      "v_lshlrev_b32 v1, 7, %[t]\n"
      "v_and_b32 v1, 0x1800, v1\n"
      "v_and_b32 v0, 0xc0, v242\n"
      "v_and_b32 v6, 24, v6\n"
      "v_and_b32 v2, 32, v2\n"
      "v_or3_b32 v1, v1, v0, v6\n"
      "v_or_b32 v1, v1, v2\n"
      "v_subrev_u32 v241, s65, v1\n"
      "s_mul_i32 s75, s73, 13\n"
      "s_sub_u32 s74, 256, s75\n"
      "s_min_i32 s74, s74, 13\n"
      "s_lshl_b32 s82, s70, 8\n"
      "s_add_u32 s75, s75, s82\n"
      "s_sub_u32 s82, 12500, s75\n"
      "s_min_i32 s74, s74, s82\n"
      "v_and_b32 v235, 15, %[t]\n"
      "v_cmp_gt_i32 s[68:69], s74, v235\n"
      "v_add_u32 v0, s75, v235\n"
      "v_mov_b32 v8, 0x7fff0000\n"
      "v_lshlrev_b32 v1, 8, v0\n"
      "v_or_b32 v2, v1, v7\n"
      "v_cndmask_b32 v246, v8, v2, s[68:69]\n"
      "v_and_b32 v1, 0xfffff000, v1\n"
      "v_lshlrev_b32 v2, 4, v0\n"
      "v_and_b32 v2, 0xf0, v2\n"
      "v_and_b32 v3, 0x300, v242\n"
      "v_or3_b32 v1, v1, v2, v3\n"
      "v_cndmask_b32 v245, v8, v1, s[68:69]\n"
      "v_lshlrev_b32 v0, 2, v0\n"
      "v_cndmask_b32 v244, v8, v0, s[68:69]\n"
      "s_mov_b32 vcc_lo, 0\n"
      "s_mov_b32 vcc_hi, 0xffff0000\n"
      "v_lshlrev_b32 v0, 2, v235\n"
      "v_cndmask_b32 v243, v8, v0, vcc\n"
      "s_add_u32 s66, s65, 0x20000\n"
      "s_mul_i32 s67, s70, 337920\n"
      "s_add_u32 s67, s67, 8716288\n"
      "s_lshl_b32 s75, s73, 8\n"
      "s_add_u32 s75, s75, s67\n"
      "s_sub_u32 s72, s71, s70\n"
      "s_lshl_b32 s72, s72, 16\n"
      "s_add_u32 s67, s67, 5120\n"
      "s_add_u32 s87, s65, 0x8000\n"
      "v_add_u32 v208, s87, v240\n"
      "v_xor_b32 v209, 16, v208\n"
      "v_add_u32 v210, s87, v241\n"
      "v_xor_b32 v211, 16, v210\n"
      "v_xor_b32 v212, 32, v210\n"
      "v_xor_b32 v213, 48, v210\n"
      "s_add_u32 s81, s65, 0x10000\n"
      "s_mov_b32 s86, s65\n"
      "s_mov_b32 s92, 3194880\n"
      "s_movk_i32 s91, 0x100\n"
      "s_mov_b32 s50, 3200000\n"
      "s_mov_b32 s51, 0x20000\n"
      "s_mov_b32 s54, 3200000\n"
      "s_mov_b32 s55, 0x20000\n"
      "s_movk_i32 s58, 25000\n"
      "s_mov_b32 s59, 0x20000\n"
      "s_lshl_b32 s90, s65, 1\n"
      "s_add_u32 s90, s90, 0x18000\n"
      "s_add_u32 s93, s90, s65\n"
      "v_add_u32 v218, s93, v241\n"
      "v_xor_b32 v219, 16, v218\n"
      "v_xor_b32 v220, 32, v218\n"
      "v_xor_b32 v221, 48, v218\n"
      "s_mov_b32 s93, 4194304\n"
      "v_and_b32 v232, 0xff, v214\n"
      "v_xor_b32 v233, 2080, v232\n"
      "v_or_b32 v232, 0x1000, v232\n"
      "v_lshlrev_b32 v234, v235, 1\n"
      "v_and_b32 v234, 0x1fff, v234\n"
      "v_lshl_or_b32 v234, v234, 16, v234\n"
      "v_sub_u32 v235, 14, v235\n"
      "v_and_b32 v236, 7, %[t]\n"
      "v_bfe_u32 v0, %[t], 3, 3\n"
      "v_lshl_or_b32 v236, v236, 3, v0\n"
      "v_mul_u32_u24 v236, 28576, v236\n"
      "v_add_u32 v236, 27262976, v236\n"
      "v_lshrrev_b32 v237, 1, v244\n"
      "s_mul_i32 s74, s71, 52\n"
      "s_lshl_b32 s82, s64, 6\n"
      "s_sub_u32 s74, s82, s74\n"
      "v_and_b32 v0, 15, %[t]\n"
      "v_lshl_or_b32 v1, v0, 6, v7\n"
      "v_add_u32 v0, s74, v0\n"
      "s_add_u32 s74, s90, 0x1000\n"
      "v_add_u32 v2, s74, v242\n"
      "v_and_b32 v3, 0x300, v242\n"
      "v_or_b32 v3, 0x18000, v3\n"
      "s_movk_i32 s74, 7984\n"
      "v_mul_u32_u24 v4, 79, v0\n"
      "v_lshl_add_u32 v5, v0, 4, v3\n"
      "v_cmp_gt_u32 vcc, 52, v0\n"
      "v_lshrrev_b32 v4, 10, v4\n"
      "v_mad_u32_u24 v4, v4, s74, v5\n"
      "v_cndmask_b32 v248, v2, v4, vcc\n"
      "v_cndmask_b32 v252, v1, v8, vcc\n"
      "v_add_u32 v0, 16, v0\n"
      "v_mul_u32_u24 v4, 79, v0\n"
      "v_lshl_add_u32 v5, v0, 4, v3\n"
      "v_cmp_gt_u32 vcc, 52, v0\n"
      "v_lshrrev_b32 v4, 10, v4\n"
      "v_mad_u32_u24 v4, v4, s74, v5\n"
      "v_cndmask_b32 v249, v2, v4, vcc\n"
      "v_cndmask_b32 v253, v1, v8, vcc\n"
      "v_add_u32 v0, 16, v0\n"
      "v_mul_u32_u24 v4, 79, v0\n"
      "v_lshl_add_u32 v5, v0, 4, v3\n"
      "v_cmp_gt_u32 vcc, 52, v0\n"
      "v_lshrrev_b32 v4, 10, v4\n"
      "v_mad_u32_u24 v4, v4, s74, v5\n"
      "v_cndmask_b32 v250, v2, v4, vcc\n"
      "v_cndmask_b32 v254, v1, v8, vcc\n"
      "v_add_u32 v0, 16, v0\n"
      "v_mul_u32_u24 v4, 79, v0\n"
      "v_lshl_add_u32 v5, v0, 4, v3\n"
      "v_cmp_gt_u32 vcc, 52, v0\n"
      "v_lshrrev_b32 v4, 10, v4\n"
      "v_mad_u32_u24 v4, v4, s74, v5\n"
      "v_cndmask_b32 v251, v2, v4, vcc\n"
      "v_cndmask_b32 v255, v1, v8, vcc\n"
      "s_mov_b32 vcc_lo, 0x7f7f7f7f\n"
      "s_mov_b32 vcc_hi, 0x7f7f7f7f\n"
      "v_bfe_u32 v222, v242, 6, 3\n"
      "v_lshl_add_u32 v222, v222, 2, s66\n"
      "v_lshrrev_b32 v223, 8, v242\n"
      "v_lshl_add_u32 v223, v223, 4, s66\n"
      "v_add_u32 v223, 2560, v223\n"
      "v_accvgpr_write_b32 a128, 0\n"
      "v_accvgpr_write_b32 a129, 0\n"
      "v_accvgpr_write_b32 a130, 0\n"
      "v_accvgpr_write_b32 a131, 0\n"
      "v_accvgpr_write_b32 a132, 0\n"
      "v_accvgpr_write_b32 a133, 0\n"
      "v_accvgpr_write_b32 a134, 0\n"
      "v_accvgpr_write_b32 a135, 0\n"
      "v_accvgpr_write_b32 a136, 0\n"
      "v_accvgpr_write_b32 a137, 0\n"
      "v_accvgpr_write_b32 a138, 0\n"
      "v_accvgpr_write_b32 a139, 0\n"
      "v_accvgpr_write_b32 a140, 0\n"
      "v_accvgpr_write_b32 a141, 0\n"
      "v_accvgpr_write_b32 a142, 0\n"
      "v_accvgpr_write_b32 a143, 0\n"
      "v_accvgpr_write_b32 a144, 0\n"
      "v_accvgpr_write_b32 a145, 0\n"
      "v_accvgpr_write_b32 a146, 0\n"
      "v_accvgpr_write_b32 a147, 0\n"
      "v_accvgpr_write_b32 a148, 0\n"
      "v_accvgpr_write_b32 a149, 0\n"
      "v_accvgpr_write_b32 a150, 0\n"
      "v_accvgpr_write_b32 a151, 0\n"
      "v_accvgpr_write_b32 a152, 0\n"
      "v_accvgpr_write_b32 a153, 0\n"
      "v_accvgpr_write_b32 a154, 0\n"
      "v_accvgpr_write_b32 a155, 0\n"
      "v_accvgpr_write_b32 a156, 0\n"
      "v_accvgpr_write_b32 a157, 0\n"
      "v_accvgpr_write_b32 a158, 0\n"
      "v_accvgpr_write_b32 a159, 0\n"
      "s_waitcnt vmcnt(12)\n"
      "buffer_load_dwordx4 a[32:35], v242, s[44:47], s78 offen offset:0\n"
      "v_cndmask_b32 v228, 0, v228, vcc\n"
      "v_lshrrev_b32 v231, 18, v228\n"
      "v_min_u32 v229, 28, v231\n"
      "v_cmp_lt_u32 vcc, 28, v231\n"
      "v_mov_b32 v230, v229\n"
      "buffer_load_dwordx4 a[36:39], v242, s[44:47], s78 offen offset:1024\n"
      "v_and_b32 v228, 0xffff, v228\n"
      "v_add_u32 v236, v228, v236\n"
      "v_add_u32_dpp v230, v230, v230 row_shr:1 row_mask:0xf bank_mask:0xf bound_ctrl:0\n"
      "s_cmp_lg_u64 vcc, 0\n"
      "s_cselect_b32 %[stat], 1, 0\n"
      "buffer_load_dwordx4 a[40:43], v242, s[44:47], s78 offen offset:2048\n"
      "v_add_u32_dpp v230, v230, v230 row_shr:2 row_mask:0xf bank_mask:0xf bound_ctrl:0\n"
      "v_cmp_eq_u32 vcc, 0, v229\n"
      "v_mov_b32 v231, 544\n"
      "v_add_u32_dpp v230, v230, v230 row_shr:4 row_mask:0xf bank_mask:0xf bound_ctrl:0\n"
      "v_lshrrev_b32 v226, 4, v242\n"
      "buffer_load_dwordx4 a[44:47], v242, s[44:47], s78 offen offset:3072\n"
      "v_mov_b32 v227, 0\n"
      "v_add_u32_dpp v230, v230, v230 row_shr:8 row_mask:0xf bank_mask:0xf bound_ctrl:0\n"
      "s_add_u32 s82, s66, 2560\n"
      "s_nop 0\n"
      "v_add_u32_dpp v230, v230, v230 row_bcast:15 row_mask:0xa bank_mask:0xf\n"
      "buffer_load_dwordx4 a[48:51], v242, s[44:47], s79 offen offset:0\n"
      "s_nop 0\n"
      "s_nop 0\n"
      "v_add_u32_dpp v230, v230, v230 row_bcast:31 row_mask:0xc bank_mask:0xf\n"
      "s_nop 0\n"
      "v_readlane_b32 s88, v230, 63\n"
      "buffer_load_dwordx4 a[52:55], v242, s[44:47], s79 offen offset:1024\n"
      "v_sub_u32 v230, v230, v229\n"
      "v_min_u32 v230, 512, v230\n"
      "v_cndmask_b32 v230, v230, v231, vcc\n"
      "s_cmp_gt_u32 s88, 512\n"
      "s_cselect_b32 %[stat], 1, %[stat]\n"
      "buffer_load_dwordx4 a[56:59], v242, s[44:47], s79 offen offset:2048\n"
      "s_cmp_lg_u32 %[stat], 0\n"
      "s_cselect_b32 s88, 0, s88\n"
      "v_lshl_add_u32 v224, v230, 2, s66\n"
      "v_lshl_add_u32 v225, v230, 1, s82\n"
      "v_add_u32 v226, s88, v226\n"
      "buffer_load_dwordx4 a[60:63], v242, s[44:47], s79 offen offset:3072\n"
      "v_lshl_add_u32 v226, v226, 1, s82\n"
      "s_add_u32 s88, s88, 31\n"
      "s_lshr_b32 s88, s88, 5\n"
      "s_max_u32 s88, s88, 1\n"
      "s_sub_u32 s82, 16, s88\n"
      "s_sub_u32 s88, s88, 1\n"
      "s_waitcnt vmcnt(16)\n"
      "s_barrier\n"
      "ds_read_b128 a[64:67], v208 offset:0\n"
      "ds_read_b128 a[68:71], v208 offset:512\n"
      "ds_read_b128 a[72:75], v208 offset:1024\n"
      "ds_read_b128 a[76:79], v208 offset:1536\n"
      "ds_read_b128 a[80:83], v209 offset:256\n"
      "ds_read_b128 a[84:87], v209 offset:768\n"
      "ds_read_b128 a[88:91], v209 offset:1280\n"
      "ds_read_b128 a[92:95], v209 offset:1792\n"
      "s_add_u32 m0, s65, 49152\n"
      "s_nop 0\n"
      "buffer_load_dwordx4 v214, s[40:43], s80 offen offset:0 lds\n"
      "buffer_load_dwordx4 v215, s[40:43], s80 offen offset:1024 lds\n"
      "buffer_load_dwordx4 v216, s[40:43], s80 offen offset:2048 lds\n"
      "buffer_load_dwordx4 v217, s[40:43], s80 offen offset:3072 lds\n"
      "s_add_u32 s80, s80, 0x4000\n"
      "v_cmp_lt_u32 vcc, 4, v229\n"
      "s_add_u32 s74, s72, s67\n"
      "s_nop 0\n"
      "v_cndmask_b32 v1, v8, v236, vcc\n"
      "v_cmp_lt_u32 vcc, 8, v229\n"
      "s_sub_u32 s74, s74, 4527104\n"
      "s_nop 0\n"
      "v_cndmask_b32 v2, v8, v236, vcc\n"
      "v_cmp_lt_u32 vcc, 12, v229\n"
      "s_add_u32 s76, s76, s74\n"
      "s_nop 0\n"
      "v_cndmask_b32 v3, v8, v236, vcc\n"
      "v_cmp_lt_u32 vcc, 16, v229\n"
      "s_add_u32 s77, s77, s74\n"
      "s_nop 0\n"
      "v_cndmask_b32 v4, v8, v236, vcc\n"
      "s_or_b32 s83, vcc_lo, vcc_hi\n"
      "v_cmp_lt_u32 vcc, 20, v229\n"
      "s_add_u32 s78, s78, s74\n"
      "s_nop 0\n"
      "v_cndmask_b32 v5, v8, v236, vcc\n"
      "s_or_b32 s84, vcc_lo, vcc_hi\n"
      "v_cmp_lt_u32 vcc, 24, v229\n"
      "s_add_u32 s79, s79, s74\n"
      "s_nop 0\n"
      "v_cndmask_b32 v6, v8, v236, vcc\n"
      "s_waitcnt vmcnt(16) lgkmcnt(4)\n"
      "v_mfma_f32_16x16x32_bf16 v[192:195], a[64:67], a[0:3], 0\n"
      "ds_read_b128 a[192:195], v208 offset:8192\n"
      "ds_read_b128 a[196:199], v208 offset:8704\n"
      "v_mfma_f32_16x16x32_bf16 v[192:195], a[68:71], a[4:7], v[192:195]\n"
      "ds_read_b128 a[200:203], v208 offset:9216\n"
      "ds_read_b128 a[204:207], v208 offset:9728\n"
      "s_add_u32 m0, s65, 65536\n"
      "v_mfma_f32_16x16x32_bf16 v[192:195], a[72:75], a[8:11], v[192:195]\n"
      "ds_read_b128 a[208:211], v209 offset:8448\n"
      "ds_read_b128 a[212:215], v209 offset:8960\n"
      "buffer_load_dwordx4 v214, s[40:43], s80 offen offset:0 lds\n"
      "v_mfma_f32_16x16x32_bf16 v[192:195], a[76:79], a[12:15], v[192:195]\n"
      "ds_read_b128 a[216:219], v209 offset:9472\n"
      "ds_read_b128 a[220:223], v209 offset:9984\n"
      "s_waitcnt lgkmcnt(8)\n"
      "v_mfma_f32_16x16x32_bf16 v[196:199], a[92:95], a[12:15], 0\n"
      "v_mfma_f32_16x16x32_bf16 v[196:199], a[88:91], a[8:11], v[196:199]\n"
      "v_mfma_f32_16x16x32_bf16 v[196:199], a[84:87], a[4:7], v[196:199]\n"
      "buffer_load_dwordx4 v215, s[40:43], s80 offen offset:1024 lds\n"
      "v_mfma_f32_16x16x32_bf16 v[196:199], a[80:83], a[0:3], v[196:199]\n"
      "v_exp_f32 v192, v192\n"
      "s_waitcnt lgkmcnt(4)\n"
      "v_mfma_f32_16x16x32_bf16 v[200:203], a[192:195], a[0:3], 0\n"
      "v_exp_f32 v193, v193\n"
      "v_mfma_f32_16x16x32_bf16 v[200:203], a[196:199], a[4:7], v[200:203]\n"
      "v_exp_f32 v194, v194\n"
      "v_cvt_pk_bf16_f32 v128, v192, v193\n"
      "v_mfma_f32_16x16x32_bf16 v[200:203], a[200:203], a[8:11], v[200:203]\n"
      "buffer_load_dwordx4 v216, s[40:43], s80 offen offset:2048 lds\n"
      "v_exp_f32 v195, v195\n"
      "v_mfma_f32_16x16x32_bf16 v[200:203], a[204:207], a[12:15], v[200:203]\n"
      "v_exp_f32 v196, v196\n"
      "v_cvt_pk_bf16_f32 v129, v194, v195\n"
      "s_waitcnt lgkmcnt(0)\n"
      "v_mfma_f32_16x16x32_bf16 v[204:207], a[220:223], a[12:15], 0\n"
      "v_exp_f32 v197, v197\n"
      "v_mfma_f32_16x16x32_bf16 v[204:207], a[216:219], a[8:11], v[204:207]\n"
      "v_exp_f32 v198, v198\n"
      "v_cvt_pk_bf16_f32 v130, v196, v197\n"
      "v_mfma_f32_16x16x32_bf16 v[204:207], a[212:215], a[4:7], v[204:207]\n"
      "buffer_load_dwordx4 v217, s[40:43], s80 offen offset:3072 lds\n"
      "v_exp_f32 v199, v199\n"
      "s_add_u32 s80, s80, 0x4000\n"
      "v_mfma_f32_16x16x32_bf16 v[204:207], a[208:211], a[0:3], v[204:207]\n"
      "v_exp_f32 v200, v200\n"
      "v_cvt_pk_bf16_f32 v131, v198, v199\n"
      "s_waitcnt vmcnt(16)\n"
      "v_mfma_f32_16x16x32_bf16 v[192:195], a[64:67], a[16:19], 0\n"
      "v_exp_f32 v201, v201\n"
      "v_mfma_f32_16x16x32_bf16 v[192:195], a[68:71], a[20:23], v[192:195]\n"
      "v_exp_f32 v202, v202\n"
      "v_cvt_pk_bf16_f32 v132, v200, v201\n"
      "v_mfma_f32_16x16x32_bf16 v[192:195], a[72:75], a[24:27], v[192:195]\n"
      "v_exp_f32 v203, v203\n"
      "v_mfma_f32_16x16x32_bf16 v[192:195], a[76:79], a[28:31], v[192:195]\n"
      "v_exp_f32 v204, v204\n"
      "v_cvt_pk_bf16_f32 v133, v202, v203\n"
      "v_mfma_f32_16x16x32_bf16 v[196:199], a[92:95], a[28:31], 0\n"
      "v_exp_f32 v205, v205\n"
      "v_mfma_f32_16x16x32_bf16 v[196:199], a[88:91], a[24:27], v[196:199]\n"
      "v_exp_f32 v206, v206\n"
      "v_cvt_pk_bf16_f32 v134, v204, v205\n"
      "v_mfma_f32_16x16x32_bf16 v[196:199], a[84:87], a[20:23], v[196:199]\n"
      "v_exp_f32 v207, v207\n"
      "v_mfma_f32_16x16x32_bf16 v[196:199], a[80:83], a[16:19], v[196:199]\n"
      "v_exp_f32 v192, v192\n"
      "v_cvt_pk_bf16_f32 v135, v206, v207\n"
      "v_mfma_f32_16x16x32_bf16 v[200:203], a[192:195], a[16:19], 0\n"
      "buffer_load_dwordx4 a[160:163], v236, s[44:47], 0 offen offset:0\n"
      "v_exp_f32 v193, v193\n"
      "v_mfma_f32_16x16x32_bf16 v[200:203], a[196:199], a[20:23], v[200:203]\n"
      "v_exp_f32 v194, v194\n"
      "v_cvt_pk_bf16_f32 v136, v192, v193\n"
      "v_mfma_f32_16x16x32_bf16 v[200:203], a[200:203], a[24:27], v[200:203]\n"
      "v_exp_f32 v195, v195\n"
      "v_mfma_f32_16x16x32_bf16 v[200:203], a[204:207], a[28:31], v[200:203]\n"
      "v_exp_f32 v196, v196\n"
      "v_cvt_pk_bf16_f32 v137, v194, v195\n"
      "v_mfma_f32_16x16x32_bf16 v[204:207], a[220:223], a[28:31], 0\n"
      "v_exp_f32 v197, v197\n"
      "v_mfma_f32_16x16x32_bf16 v[204:207], a[216:219], a[24:27], v[204:207]\n"
      "v_exp_f32 v198, v198\n"
      "v_cvt_pk_bf16_f32 v138, v196, v197\n"
      "v_mfma_f32_16x16x32_bf16 v[204:207], a[212:215], a[20:23], v[204:207]\n"
      "buffer_load_dwordx4 a[164:167], v1, s[44:47], 0 offen offset:16\n"
      "v_exp_f32 v199, v199\n"
      "v_mfma_f32_16x16x32_bf16 v[204:207], a[208:211], a[16:19], v[204:207]\n"
      "v_exp_f32 v200, v200\n"
      "v_cvt_pk_bf16_f32 v139, v198, v199\n"
      "s_waitcnt vmcnt(14)\n"
      "v_mfma_f32_16x16x32_bf16 v[192:195], a[64:67], a[32:35], 0\n"
      "v_exp_f32 v201, v201\n"
      "v_mfma_f32_16x16x32_bf16 v[192:195], a[68:71], a[36:39], v[192:195]\n"
      "v_exp_f32 v202, v202\n"
      "v_cvt_pk_bf16_f32 v140, v200, v201\n"
      "v_mfma_f32_16x16x32_bf16 v[192:195], a[72:75], a[40:43], v[192:195]\n"
      "ds_read_b64_tr_b16 a[96:97], v210 offset:0\n"
      "v_exp_f32 v203, v203\n"
      "v_mfma_f32_16x16x32_bf16 v[192:195], a[76:79], a[44:47], v[192:195]\n"
      "v_exp_f32 v204, v204\n"
      "v_cvt_pk_bf16_f32 v141, v202, v203\n"
      "v_mfma_f32_16x16x32_bf16 v[196:199], a[92:95], a[44:47], 0\n"
      "buffer_load_dwordx4 a[168:171], v2, s[44:47], 0 offen offset:32\n"
      "ds_read_b64_tr_b16 a[98:99], v211 offset:256\n"
      "v_exp_f32 v205, v205\n"
      "v_mfma_f32_16x16x32_bf16 v[196:199], a[88:91], a[40:43], v[196:199]\n"
      "v_exp_f32 v206, v206\n"
      "v_cvt_pk_bf16_f32 v142, v204, v205\n"
      "v_mfma_f32_16x16x32_bf16 v[196:199], a[84:87], a[36:39], v[196:199]\n"
      "ds_read_b64_tr_b16 a[100:101], v210 offset:8192\n"
      "v_exp_f32 v207, v207\n"
      "v_mfma_f32_16x16x32_bf16 v[196:199], a[80:83], a[32:35], v[196:199]\n"
      "v_exp_f32 v192, v192\n"
      "v_cvt_pk_bf16_f32 v143, v206, v207\n"
      "v_mfma_f32_16x16x32_bf16 v[200:203], a[192:195], a[32:35], 0\n"
      "ds_read_b64_tr_b16 a[102:103], v211 offset:8448\n"
      "v_exp_f32 v193, v193\n"
      "v_mfma_f32_16x16x32_bf16 v[200:203], a[196:199], a[36:39], v[200:203]\n"
      "v_exp_f32 v194, v194\n"
      "v_cvt_pk_bf16_f32 v144, v192, v193\n"
      "v_mfma_f32_16x16x32_bf16 v[200:203], a[200:203], a[40:43], v[200:203]\n"
      "buffer_load_dwordx4 a[172:175], v3, s[44:47], 0 offen offset:48\n"
      "ds_read_b64_tr_b16 a[104:105], v212 offset:0\n"
      "v_exp_f32 v195, v195\n"
      "v_mfma_f32_16x16x32_bf16 v[200:203], a[204:207], a[44:47], v[200:203]\n"
      "v_exp_f32 v196, v196\n"
      "v_cvt_pk_bf16_f32 v145, v194, v195\n"
      "v_mfma_f32_16x16x32_bf16 v[204:207], a[220:223], a[44:47], 0\n"
      "ds_read_b64_tr_b16 a[106:107], v213 offset:256\n"
      "v_exp_f32 v197, v197\n"
      "v_mfma_f32_16x16x32_bf16 v[204:207], a[216:219], a[40:43], v[204:207]\n"
      "v_exp_f32 v198, v198\n"
      "v_cvt_pk_bf16_f32 v146, v196, v197\n"
      "v_mfma_f32_16x16x32_bf16 v[204:207], a[212:215], a[36:39], v[204:207]\n"
      "ds_read_b64_tr_b16 a[108:109], v212 offset:8192\n"
      "v_exp_f32 v199, v199\n"
      "v_mfma_f32_16x16x32_bf16 v[204:207], a[208:211], a[32:35], v[204:207]\n"
      "v_exp_f32 v200, v200\n"
      "v_cvt_pk_bf16_f32 v147, v198, v199\n"
      "s_waitcnt vmcnt(12)\n"
      "v_mfma_f32_16x16x32_bf16 v[192:195], a[64:67], a[48:51], 0\n"
      "buffer_load_dwordx4 a[176:179], v4, s[44:47], 0 offen offset:64\n"
      "ds_read_b64_tr_b16 a[110:111], v213 offset:8448\n"
      "v_exp_f32 v201, v201\n"
      "v_mfma_f32_16x16x32_bf16 v[192:195], a[68:71], a[52:55], v[192:195]\n"
      "v_exp_f32 v202, v202\n"
      "v_cvt_pk_bf16_f32 v148, v200, v201\n"
      "v_mfma_f32_16x16x32_bf16 v[192:195], a[72:75], a[56:59], v[192:195]\n"
      "v_exp_f32 v203, v203\n"
      "v_mfma_f32_16x16x32_bf16 v[192:195], a[76:79], a[60:63], v[192:195]\n"
      "v_exp_f32 v204, v204\n"
      "v_cvt_pk_bf16_f32 v149, v202, v203\n"
      "s_waitcnt vmcnt(9)\n"
      "s_barrier\n"
      "v_mfma_f32_16x16x32_bf16 v[196:199], a[92:95], a[60:63], 0\n"
      "v_exp_f32 v205, v205\n"
      "v_mfma_f32_16x16x32_bf16 v[196:199], a[88:91], a[56:59], v[196:199]\n"
      "ds_read_b128 a[64:67], v208 offset:16384\n"
      "v_exp_f32 v206, v206\n"
      "v_cvt_pk_bf16_f32 v150, v204, v205\n"
      "v_mfma_f32_16x16x32_bf16 v[196:199], a[84:87], a[52:55], v[196:199]\n"
      "buffer_load_dwordx4 a[180:183], v5, s[44:47], 0 offen offset:80\n"
      "ds_read_b128 a[68:71], v208 offset:16896\n"
      "v_exp_f32 v207, v207\n"
      "v_mfma_f32_16x16x32_bf16 v[196:199], a[80:83], a[48:51], v[196:199]\n"
      "ds_read_b128 a[72:75], v208 offset:17408\n"
      "v_exp_f32 v192, v192\n"
      "v_cvt_pk_bf16_f32 v151, v206, v207\n"
      "v_mfma_f32_16x16x32_bf16 v[200:203], a[192:195], a[48:51], 0\n"
      "ds_read_b128 a[76:79], v208 offset:17920\n"
      "v_exp_f32 v193, v193\n"
      "v_mfma_f32_16x16x32_bf16 v[200:203], a[196:199], a[52:55], v[200:203]\n"
      "v_exp_f32 v194, v194\n"
      "v_cvt_pk_bf16_f32 v152, v192, v193\n"
      "v_mfma_f32_16x16x32_bf16 v[200:203], a[200:203], a[56:59], v[200:203]\n"
      "v_exp_f32 v195, v195\n"
      "v_mfma_f32_16x16x32_bf16 v[200:203], a[204:207], a[60:63], v[200:203]\n"
      "v_exp_f32 v196, v196\n"
      "v_cvt_pk_bf16_f32 v153, v194, v195\n"
      "v_mfma_f32_16x16x32_bf16 v[204:207], a[220:223], a[60:63], 0\n"
      "buffer_load_dwordx4 a[184:187], v6, s[44:47], 0 offen offset:96\n"
      "v_exp_f32 v197, v197\n"
      "v_mfma_f32_16x16x32_bf16 v[204:207], a[216:219], a[56:59], v[204:207]\n"
      "v_exp_f32 v198, v198\n"
      "v_cvt_pk_bf16_f32 v154, v196, v197\n"
      "v_mfma_f32_16x16x32_bf16 v[204:207], a[212:215], a[52:55], v[204:207]\n"
      "v_exp_f32 v199, v199\n"
      "v_mfma_f32_16x16x32_bf16 v[204:207], a[208:211], a[48:51], v[204:207]\n"
      "v_exp_f32 v200, v200\n"
      "v_cvt_pk_bf16_f32 v155, v198, v199\n"
      "v_exp_f32 v201, v201\n"
      "v_exp_f32 v202, v202\n"
      "v_cvt_pk_bf16_f32 v156, v200, v201\n"
      "v_exp_f32 v203, v203\n"
      "s_waitcnt lgkmcnt(0)\n"
      "s_nop 0\n"
      "v_exp_f32 v204, v204\n"
      "v_cvt_pk_bf16_f32 v157, v202, v203\n"
      "v_mfma_f32_16x16x32_bf16 v[192:195], a[64:67], a[0:3], 0\n"
      "ds_read_b128 a[80:83], v209 offset:16640\n"
      "s_add_u32 m0, s65, 81920\n"
      "v_mfma_f32_16x16x32_bf16 v[192:195], a[68:71], a[4:7], v[192:195]\n"
      "v_exp_f32 v205, v205\n"
      "v_mfma_f32_16x16x32_bf16 v[192:195], a[72:75], a[8:11], v[192:195]\n"
      "ds_read_b64_tr_b16 a[112:113], v210 offset:512\n"
      "v_cvt_pk_bf16_f32 v158, v204, v205\n"
      "v_mfma_f32_16x16x32_bf16 v[192:195], a[76:79], a[12:15], v[192:195]\n"
      "v_exp_f32 v206, v206\n"
      "v_mfma_f32_16x16x32_bf16 v[196:199], a[76:79], a[28:31], 0\n"
      "ds_read_b128 a[84:87], v209 offset:17152\n"
      "buffer_load_dwordx4 v214, s[40:43], s80 offen offset:0 lds\n"
      "v_mfma_f32_16x16x32_bf16 v[196:199], a[72:75], a[24:27], v[196:199]\n"
      "v_exp_f32 v207, v207\n"
      "v_mfma_f32_16x16x32_bf16 v[196:199], a[68:71], a[20:23], v[196:199]\n"
      "ds_read_b64_tr_b16 a[114:115], v211 offset:768\n"
      "v_cvt_pk_bf16_f32 v159, v206, v207\n"
      "v_mfma_f32_16x16x32_bf16 v[196:199], a[64:67], a[16:19], v[196:199]\n"
      "v_exp_f32 v192, v192\n"
      "v_mfma_f32_16x16x32_bf16 v[200:203], a[64:67], a[32:35], 0\n"
      "ds_read_b128 a[88:91], v209 offset:17664\n"
      "buffer_load_ushort a224, v237, s[56:59], 0 offen\n"
      "v_mfma_f32_16x16x32_bf16 v[200:203], a[68:71], a[36:39], v[200:203]\n"
      "v_exp_f32 v193, v193\n"
      "v_mfma_f32_16x16x32_bf16 v[200:203], a[72:75], a[40:43], v[200:203]\n"
      "ds_read_b64_tr_b16 a[116:117], v210 offset:8704\n"
      "v_cvt_pk_bf16_f32 v160, v192, v193\n"
      "v_mfma_f32_16x16x32_bf16 v[200:203], a[76:79], a[44:47], v[200:203]\n"
      "v_exp_f32 v194, v194\n"
      "v_mfma_f32_16x16x32_bf16 v[204:207], a[76:79], a[60:63], 0\n"
      "ds_read_b128 a[92:95], v209 offset:18176\n"
      "buffer_load_dwordx4 v215, s[40:43], s80 offen offset:1024 lds\n"
      "s_waitcnt lgkmcnt(7)\n"
      "v_mfma_f32_16x16x32_bf16 v[204:207], a[72:75], a[56:59], v[204:207]\n"
      "v_exp_f32 v195, v195\n"
      "v_mfma_f32_16x16x32_bf16 v[204:207], a[68:71], a[52:55], v[204:207]\n"
      "ds_read_b64_tr_b16 a[118:119], v211 offset:8960\n"
      "v_cvt_pk_bf16_f32 v161, v194, v195\n"
      "v_mfma_f32_16x16x32_bf16 v[204:207], a[64:67], a[48:51], v[204:207]\n"
      "v_exp_f32 v196, v196\n"
      "v_mfma_f32_16x16x32_bf16 v[0:3], a[96:99], v[128:131], 0\n"
      "ds_read_b64_tr_b16 a[120:121], v212 offset:512\n"
      "v_mfma_f32_16x16x32_bf16 v[0:3], a[100:103], v[132:135], v[0:3]\n"
      "v_exp_f32 v197, v197\n"
      "v_mfma_f32_16x16x32_bf16 v[32:35], a[100:103], v[140:143], 0\n"
      "ds_read_b64_tr_b16 a[122:123], v213 offset:768\n"
      "v_cvt_pk_bf16_f32 v168, v196, v197\n"
      "v_mfma_f32_16x16x32_bf16 v[32:35], a[96:99], v[136:139], v[32:35]\n"
      "v_exp_f32 v198, v198\n"
      "v_mfma_f32_16x16x32_bf16 v[64:67], a[96:99], v[144:147], 0\n"
      "ds_read_b64_tr_b16 a[124:125], v212 offset:8704\n"
      "buffer_load_dwordx4 v216, s[40:43], s80 offen offset:2048 lds\n"
      "v_mfma_f32_16x16x32_bf16 v[64:67], a[100:103], v[148:151], v[64:67]\n"
      "v_exp_f32 v199, v199\n"
      "v_mfma_f32_16x16x32_bf16 v[96:99], a[100:103], v[156:159], 0\n"
      "ds_read_b64_tr_b16 a[126:127], v213 offset:8960\n"
      "v_cvt_pk_bf16_f32 v169, v198, v199\n"
      "v_mfma_f32_16x16x32_bf16 v[96:99], a[96:99], v[152:155], v[96:99]\n"
      "v_exp_f32 v200, v200\n"
      "v_mfma_f32_16x16x32_bf16 v[100:103], a[104:107], v[152:155], 0\n"
      "v_mfma_f32_16x16x32_bf16 v[100:103], a[108:111], v[156:159], v[100:103]\n"
      "v_exp_f32 v201, v201\n"
      "v_mfma_f32_16x16x32_bf16 v[68:71], a[108:111], v[148:151], 0\n"
      "v_cvt_pk_bf16_f32 v176, v200, v201\n"
      "v_mfma_f32_16x16x32_bf16 v[68:71], a[104:107], v[144:147], v[68:71]\n"
      "v_exp_f32 v202, v202\n"
      "v_mfma_f32_16x16x32_bf16 v[36:39], a[104:107], v[136:139], 0\n"
      "buffer_load_dwordx4 v217, s[40:43], s80 offen offset:3072 lds\n"
      "s_add_u32 s80, s80, 0x4000\n"
      "s_cmp_eq_u32 s83, 0\n"
      "v_mfma_f32_16x16x32_bf16 v[36:39], a[108:111], v[140:143], v[36:39]\n"
      "v_exp_f32 v203, v203\n"
      "v_mfma_f32_16x16x32_bf16 v[4:7], a[108:111], v[132:135], 0\n"
      "v_cvt_pk_bf16_f32 v177, v202, v203\n"
      "s_waitcnt vmcnt(5) lgkmcnt(5)\n"
      "s_cbranch_scc0 12f\n"
      "11:\n"
      "v_mfma_f32_16x16x32_bf16 v[4:7], a[104:107], v[128:131], v[4:7]\n"
      "v_exp_f32 v204, v204\n"
      "v_mfma_f32_16x16x32_bf16 v[192:195], a[80:83], a[0:3], 0\n"
      "ds_read_b128 a[64:67], v208 offset:24576\n"
      "ds_write_b32 v224, a175 offset:60\n"
      "s_add_u32 m0, s65, 0\n"
      "v_mfma_f32_16x16x32_bf16 v[192:195], a[84:87], a[4:7], v[192:195]\n"
      "v_exp_f32 v205, v205\n"
      "v_mfma_f32_16x16x32_bf16 v[192:195], a[88:91], a[8:11], v[192:195]\n"
      "ds_read_b64_tr_b16 a[96:97], v210 offset:1024\n"
      "v_cvt_pk_bf16_f32 v184, v204, v205\n"
      "v_mfma_f32_16x16x32_bf16 v[192:195], a[92:95], a[12:15], v[192:195]\n"
      "ds_write_b16_d16_hi v225, a175 offset:30\n"
      "v_exp_f32 v206, v206\n"
      "v_mfma_f32_16x16x32_bf16 v[196:199], a[92:95], a[28:31], 0\n"
      "ds_read_b128 a[68:71], v208 offset:25088\n"
      "buffer_load_dwordx4 v214, s[40:43], s80 offen offset:0 lds\n"
      "v_mfma_f32_16x16x32_bf16 v[196:199], a[88:91], a[24:27], v[196:199]\n"
      "v_exp_f32 v207, v207\n"
      "v_mfma_f32_16x16x32_bf16 v[196:199], a[84:87], a[20:23], v[196:199]\n"
      "ds_read_b64_tr_b16 a[98:99], v211 offset:1280\n"
      "v_cvt_pk_bf16_f32 v185, v206, v207\n"
      "v_mfma_f32_16x16x32_bf16 v[196:199], a[80:83], a[16:19], v[196:199]\n"
      "ds_write_b32 v224, a174 offset:56\n"
      "v_exp_f32 v192, v192\n"
      "v_mfma_f32_16x16x32_bf16 v[200:203], a[80:83], a[32:35], 0\n"
      "ds_read_b128 a[72:75], v208 offset:25600\n"
      "ds_write_b16_d16_hi v225, a174 offset:28\n"
      "v_mfma_f32_16x16x32_bf16 v[200:203], a[84:87], a[36:39], v[200:203]\n"
      "v_exp_f32 v193, v193\n"
      "v_mfma_f32_16x16x32_bf16 v[200:203], a[88:91], a[40:43], v[200:203]\n"
      "ds_read_b64_tr_b16 a[100:101], v210 offset:9216\n"
      "v_cvt_pk_bf16_f32 v162, v192, v193\n"
      "v_mfma_f32_16x16x32_bf16 v[200:203], a[92:95], a[44:47], v[200:203]\n"
      "ds_write_b32 v224, a173 offset:52\n"
      "v_exp_f32 v194, v194\n"
      "v_mfma_f32_16x16x32_bf16 v[204:207], a[92:95], a[60:63], 0\n"
      "ds_read_b128 a[76:79], v208 offset:26112\n"
      "buffer_load_dwordx4 v215, s[40:43], s80 offen offset:1024 lds\n"
      "s_waitcnt lgkmcnt(12)\n"
      "v_mfma_f32_16x16x32_bf16 v[204:207], a[88:91], a[56:59], v[204:207]\n"
      "v_exp_f32 v195, v195\n"
      "v_mfma_f32_16x16x32_bf16 v[204:207], a[84:87], a[52:55], v[204:207]\n"
      "ds_read_b64_tr_b16 a[102:103], v211 offset:9472\n"
      "v_cvt_pk_bf16_f32 v163, v194, v195\n"
      "v_mfma_f32_16x16x32_bf16 v[204:207], a[80:83], a[48:51], v[204:207]\n"
      "ds_write_b16_d16_hi v225, a173 offset:26\n"
      "v_exp_f32 v196, v196\n"
      "v_mfma_f32_16x16x32_bf16 v[8:11], a[112:115], v[128:131], 0\n"
      "ds_read_b64_tr_b16 a[104:105], v212 offset:1024\n"
      "ds_write_b32 v224, a172 offset:48\n"
      "v_mfma_f32_16x16x32_bf16 v[8:11], a[116:119], v[132:135], v[8:11]\n"
      "v_exp_f32 v197, v197\n"
      "v_mfma_f32_16x16x32_bf16 v[40:43], a[116:119], v[140:143], 0\n"
      "ds_read_b64_tr_b16 a[106:107], v213 offset:1280\n"
      "v_cvt_pk_bf16_f32 v170, v196, v197\n"
      "v_mfma_f32_16x16x32_bf16 v[40:43], a[112:115], v[136:139], v[40:43]\n"
      "ds_write_b16_d16_hi v225, a172 offset:24\n"
      "v_exp_f32 v198, v198\n"
      "v_mfma_f32_16x16x32_bf16 v[72:75], a[112:115], v[144:147], 0\n"
      "ds_read_b64_tr_b16 a[108:109], v212 offset:9216\n"
      "buffer_load_dwordx4 v216, s[40:43], s80 offen offset:2048 lds\n"
      "v_mfma_f32_16x16x32_bf16 v[72:75], a[116:119], v[148:151], v[72:75]\n"
      "v_exp_f32 v199, v199\n"
      "v_mfma_f32_16x16x32_bf16 v[104:107], a[116:119], v[156:159], 0\n"
      "ds_read_b64_tr_b16 a[110:111], v213 offset:9472\n"
      "v_cvt_pk_bf16_f32 v171, v198, v199\n"
      "v_mfma_f32_16x16x32_bf16 v[104:107], a[112:115], v[152:155], v[104:107]\n"
      "ds_write_b32 v224, a171 offset:44\n"
      "v_exp_f32 v200, v200\n"
      "v_mfma_f32_16x16x32_bf16 v[108:111], a[120:123], v[152:155], 0\n"
      "ds_write_b16_d16_hi v225, a171 offset:22\n"
      "v_mfma_f32_16x16x32_bf16 v[108:111], a[124:127], v[156:159], v[108:111]\n"
      "v_exp_f32 v201, v201\n"
      "v_mfma_f32_16x16x32_bf16 v[76:79], a[124:127], v[148:151], 0\n"
      "ds_write_b32 v224, a170 offset:40\n"
      "v_cvt_pk_bf16_f32 v178, v200, v201\n"
      "v_mfma_f32_16x16x32_bf16 v[76:79], a[120:123], v[144:147], v[76:79]\n"
      "v_exp_f32 v202, v202\n"
      "v_mfma_f32_16x16x32_bf16 v[44:47], a[120:123], v[136:139], 0\n"
      "buffer_load_dwordx4 v217, s[40:43], s80 offen offset:3072 lds\n"
      "s_add_u32 s80, s80, 0x4000\n"
      "v_mfma_f32_16x16x32_bf16 v[44:47], a[124:127], v[140:143], v[44:47]\n"
      "v_exp_f32 v203, v203\n"
      "v_mfma_f32_16x16x32_bf16 v[12:15], a[124:127], v[132:135], 0\n"
      "v_cvt_pk_bf16_f32 v179, v202, v203\n"
      "s_waitcnt lgkmcnt(11)\n"
      "v_mfma_f32_16x16x32_bf16 v[12:15], a[120:123], v[128:131], v[12:15]\n"
      "v_exp_f32 v204, v204\n"
      "v_mfma_f32_16x16x32_bf16 v[192:195], a[64:67], a[0:3], 0\n"
      "ds_read_b128 a[80:83], v209 offset:24832\n"
      "ds_write_b16_d16_hi v225, a170 offset:20\n"
      "v_mfma_f32_16x16x32_bf16 v[192:195], a[68:71], a[4:7], v[192:195]\n"
      "v_exp_f32 v205, v205\n"
      "v_mfma_f32_16x16x32_bf16 v[192:195], a[72:75], a[8:11], v[192:195]\n"
      "ds_read_b64_tr_b16 a[112:113], v210 offset:1536\n"
      "v_cvt_pk_bf16_f32 v186, v204, v205\n"
      "v_mfma_f32_16x16x32_bf16 v[192:195], a[76:79], a[12:15], v[192:195]\n"
      "ds_write_b32 v224, a169 offset:36\n"
      "v_exp_f32 v206, v206\n"
      "v_mfma_f32_16x16x32_bf16 v[196:199], a[76:79], a[28:31], 0\n"
      "ds_read_b128 a[84:87], v209 offset:25344\n"
      "buffer_load_dwordx4 a[192:195], v245, s[44:47], s93 offen offset:0\n"
      "v_mfma_f32_16x16x32_bf16 v[196:199], a[72:75], a[24:27], v[196:199]\n"
      "v_exp_f32 v207, v207\n"
      "v_mfma_f32_16x16x32_bf16 v[196:199], a[68:71], a[20:23], v[196:199]\n"
      "ds_read_b64_tr_b16 a[114:115], v211 offset:1792\n"
      "v_cvt_pk_bf16_f32 v187, v206, v207\n"
      "v_mfma_f32_16x16x32_bf16 v[196:199], a[64:67], a[16:19], v[196:199]\n"
      "ds_write_b16_d16_hi v225, a169 offset:18\n"
      "v_exp_f32 v192, v192\n"
      "v_mfma_f32_16x16x32_bf16 v[200:203], a[64:67], a[32:35], 0\n"
      "ds_read_b128 a[88:91], v209 offset:25856\n"
      "ds_write_b32 v224, a168 offset:32\n"
      "v_mfma_f32_16x16x32_bf16 v[200:203], a[68:71], a[36:39], v[200:203]\n"
      "v_exp_f32 v193, v193\n"
      "v_mfma_f32_16x16x32_bf16 v[200:203], a[72:75], a[40:43], v[200:203]\n"
      "ds_read_b64_tr_b16 a[116:117], v210 offset:9728\n"
      "v_cvt_pk_bf16_f32 v164, v192, v193\n"
      "v_mfma_f32_16x16x32_bf16 v[200:203], a[76:79], a[44:47], v[200:203]\n"
      "ds_write_b16_d16_hi v225, a168 offset:16\n"
      "v_exp_f32 v194, v194\n"
      "v_mfma_f32_16x16x32_bf16 v[204:207], a[76:79], a[60:63], 0\n"
      "ds_read_b128 a[92:95], v209 offset:26368\n"
      "buffer_load_dwordx4 a[196:199], v245, s[44:47], s93 offen offset:1024\n"
      "s_waitcnt lgkmcnt(14)\n"
      "v_mfma_f32_16x16x32_bf16 v[204:207], a[72:75], a[56:59], v[204:207]\n"
      "v_exp_f32 v195, v195\n"
      "v_mfma_f32_16x16x32_bf16 v[204:207], a[68:71], a[52:55], v[204:207]\n"
      "ds_read_b64_tr_b16 a[118:119], v211 offset:9984\n"
      "v_cvt_pk_bf16_f32 v165, v194, v195\n"
      "v_mfma_f32_16x16x32_bf16 v[204:207], a[64:67], a[48:51], v[204:207]\n"
      "ds_write_b32 v224, a167 offset:28\n"
      "v_exp_f32 v196, v196\n"
      "v_mfma_f32_16x16x32_bf16 v[16:19], a[96:99], v[128:131], 0\n"
      "ds_read_b64_tr_b16 a[120:121], v212 offset:1536\n"
      "ds_write_b16_d16_hi v225, a167 offset:14\n"
      "v_add_u32 v208, s81, v240\n"
      "v_mfma_f32_16x16x32_bf16 v[16:19], a[100:103], v[132:135], v[16:19]\n"
      "v_exp_f32 v197, v197\n"
      "v_mfma_f32_16x16x32_bf16 v[48:51], a[100:103], v[140:143], 0\n"
      "ds_read_b64_tr_b16 a[122:123], v213 offset:1792\n"
      "v_cvt_pk_bf16_f32 v172, v196, v197\n"
      "v_mfma_f32_16x16x32_bf16 v[48:51], a[96:99], v[136:139], v[48:51]\n"
      "ds_write_b32 v224, a166 offset:24\n"
      "v_exp_f32 v198, v198\n"
      "v_mfma_f32_16x16x32_bf16 v[80:83], a[96:99], v[144:147], 0\n"
      "ds_read_b64_tr_b16 a[124:125], v212 offset:9728\n"
      "buffer_load_dwordx4 a[200:203], v245, s[44:47], s93 offen offset:2048\n"
      "v_mfma_f32_16x16x32_bf16 v[80:83], a[100:103], v[148:151], v[80:83]\n"
      "v_exp_f32 v199, v199\n"
      "v_mfma_f32_16x16x32_bf16 v[112:115], a[100:103], v[156:159], 0\n"
      "ds_read_b64_tr_b16 a[126:127], v213 offset:9984\n"
      "v_cvt_pk_bf16_f32 v173, v198, v199\n"
      "v_mfma_f32_16x16x32_bf16 v[112:115], a[96:99], v[152:155], v[112:115]\n"
      "ds_write_b16_d16_hi v225, a166 offset:12\n"
      "v_exp_f32 v200, v200\n"
      "v_mfma_f32_16x16x32_bf16 v[116:119], a[104:107], v[152:155], 0\n"
      "ds_write_b32 v224, a165 offset:20\n"
      "v_mfma_f32_16x16x32_bf16 v[116:119], a[108:111], v[156:159], v[116:119]\n"
      "v_exp_f32 v201, v201\n"
      "v_mfma_f32_16x16x32_bf16 v[84:87], a[108:111], v[148:151], 0\n"
      "ds_write_b16_d16_hi v225, a165 offset:10\n"
      "v_cvt_pk_bf16_f32 v180, v200, v201\n"
      "v_xor_b32 v209, 16, v208\n"
      "v_mfma_f32_16x16x32_bf16 v[84:87], a[104:107], v[144:147], v[84:87]\n"
      "v_exp_f32 v202, v202\n"
      "v_mfma_f32_16x16x32_bf16 v[52:55], a[104:107], v[136:139], 0\n"
      "buffer_load_dwordx4 a[204:207], v245, s[44:47], s93 offen offset:3072\n"
      "v_mfma_f32_16x16x32_bf16 v[52:55], a[108:111], v[140:143], v[52:55]\n"
      "v_exp_f32 v203, v203\n"
      "v_mfma_f32_16x16x32_bf16 v[20:23], a[108:111], v[132:135], 0\n"
      "v_cvt_pk_bf16_f32 v181, v202, v203\n"
      "s_waitcnt lgkmcnt(11)\n"
      "s_barrier\n"
      "v_mfma_f32_16x16x32_bf16 v[20:23], a[104:107], v[128:131], v[20:23]\n"
      "v_exp_f32 v204, v204\n"
      "v_mfma_f32_16x16x32_bf16 v[192:195], a[80:83], a[0:3], 0\n"
      "ds_read_b128 a[64:67], v208 offset:0\n"
      "ds_write_b32 v224, a164 offset:16\n"
      "v_mfma_f32_16x16x32_bf16 v[192:195], a[84:87], a[4:7], v[192:195]\n"
      "v_exp_f32 v205, v205\n"
      "v_mfma_f32_16x16x32_bf16 v[192:195], a[88:91], a[8:11], v[192:195]\n"
      "ds_read_b64_tr_b16 a[96:97], v210 offset:16384\n"
      "v_cvt_pk_bf16_f32 v188, v204, v205\n"
      "v_mfma_f32_16x16x32_bf16 v[192:195], a[92:95], a[12:15], v[192:195]\n"
      "ds_write_b16_d16_hi v225, a164 offset:8\n"
      "v_exp_f32 v206, v206\n"
      "v_mfma_f32_16x16x32_bf16 v[196:199], a[92:95], a[28:31], 0\n"
      "ds_read_b128 a[68:71], v208 offset:512\n"
      "buffer_load_dwordx4 a[208:211], v246, s[48:51], 0 offen offset:0\n"
      "v_mfma_f32_16x16x32_bf16 v[196:199], a[88:91], a[24:27], v[196:199]\n"
      "v_exp_f32 v207, v207\n"
      "v_mfma_f32_16x16x32_bf16 v[196:199], a[84:87], a[20:23], v[196:199]\n"
      "ds_read_b64_tr_b16 a[98:99], v211 offset:16640\n"
      "v_cvt_pk_bf16_f32 v189, v206, v207\n"
      "v_mfma_f32_16x16x32_bf16 v[196:199], a[80:83], a[16:19], v[196:199]\n"
      "ds_write_b32 v224, a163 offset:12\n"
      "v_exp_f32 v192, v192\n"
      "v_mfma_f32_16x16x32_bf16 v[200:203], a[80:83], a[32:35], 0\n"
      "ds_read_b128 a[72:75], v208 offset:1024\n"
      "ds_write_b16_d16_hi v225, a163 offset:6\n"
      "v_mfma_f32_16x16x32_bf16 v[200:203], a[84:87], a[36:39], v[200:203]\n"
      "v_exp_f32 v193, v193\n"
      "v_mfma_f32_16x16x32_bf16 v[200:203], a[88:91], a[40:43], v[200:203]\n"
      "ds_read_b64_tr_b16 a[100:101], v210 offset:24576\n"
      "v_cvt_pk_bf16_f32 v166, v192, v193\n"
      "v_mfma_f32_16x16x32_bf16 v[200:203], a[92:95], a[44:47], v[200:203]\n"
      "ds_write_b32 v224, a162 offset:8\n"
      "v_exp_f32 v194, v194\n"
      "v_mfma_f32_16x16x32_bf16 v[204:207], a[92:95], a[60:63], 0\n"
      "ds_read_b128 a[76:79], v208 offset:1536\n"
      "buffer_load_dwordx4 a[212:215], v246, s[48:51], 0 offen offset:64\n"
      "s_waitcnt lgkmcnt(14)\n"
      "v_mfma_f32_16x16x32_bf16 v[204:207], a[88:91], a[56:59], v[204:207]\n"
      "v_exp_f32 v195, v195\n"
      "v_mfma_f32_16x16x32_bf16 v[204:207], a[84:87], a[52:55], v[204:207]\n"
      "ds_read_b64_tr_b16 a[102:103], v211 offset:24832\n"
      "v_cvt_pk_bf16_f32 v167, v194, v195\n"
      "v_mfma_f32_16x16x32_bf16 v[204:207], a[80:83], a[48:51], v[204:207]\n"
      "ds_write_b16_d16_hi v225, a162 offset:4\n"
      "v_exp_f32 v196, v196\n"
      "v_mfma_f32_16x16x32_bf16 v[24:27], a[112:115], v[128:131], 0\n"
      "ds_read_b64_tr_b16 a[104:105], v212 offset:16384\n"
      "ds_write_b32 v224, a161 offset:4\n"
      "v_mfma_f32_16x16x32_bf16 v[24:27], a[116:119], v[132:135], v[24:27]\n"
      "v_exp_f32 v197, v197\n"
      "v_mfma_f32_16x16x32_bf16 v[56:59], a[116:119], v[140:143], 0\n"
      "ds_read_b64_tr_b16 a[106:107], v213 offset:16640\n"
      "v_cvt_pk_bf16_f32 v174, v196, v197\n"
      "v_mfma_f32_16x16x32_bf16 v[56:59], a[112:115], v[136:139], v[56:59]\n"
      "ds_write_b16_d16_hi v225, a161 offset:2\n"
      "v_exp_f32 v198, v198\n"
      "v_mfma_f32_16x16x32_bf16 v[88:91], a[112:115], v[144:147], 0\n"
      "ds_read_b64_tr_b16 a[108:109], v212 offset:24576\n"
      "s_add_u32 m0, s86, 0x4000\n"
      "v_mfma_f32_16x16x32_bf16 v[88:91], a[116:119], v[148:151], v[88:91]\n"
      "v_exp_f32 v199, v199\n"
      "v_mfma_f32_16x16x32_bf16 v[120:123], a[116:119], v[156:159], 0\n"
      "ds_read_b64_tr_b16 a[110:111], v213 offset:24832\n"
      "v_cvt_pk_bf16_f32 v175, v198, v199\n"
      "v_mfma_f32_16x16x32_bf16 v[120:123], a[112:115], v[152:155], v[120:123]\n"
      "ds_write_b32 v224, a160 offset:0\n"
      "v_exp_f32 v200, v200\n"
      "v_mfma_f32_16x16x32_bf16 v[124:127], a[120:123], v[152:155], 0\n"
      "buffer_load_dwordx4 v214, s[40:43], s80 offen offset:0 lds\n"
      "ds_write_b16_d16_hi v225, a160 offset:0\n"
      "v_mfma_f32_16x16x32_bf16 v[124:127], a[124:127], v[156:159], v[124:127]\n"
      "v_exp_f32 v201, v201\n"
      "v_mfma_f32_16x16x32_bf16 v[92:95], a[124:127], v[148:151], 0\n"
      "ds_write_b16 v226, v227\n"
      "v_cvt_pk_bf16_f32 v182, v200, v201\n"
      "v_mfma_f32_16x16x32_bf16 v[92:95], a[120:123], v[144:147], v[92:95]\n"
      "v_exp_f32 v202, v202\n"
      "v_mfma_f32_16x16x32_bf16 v[60:63], a[120:123], v[136:139], 0\n"
      "buffer_load_dwordx4 v215, s[40:43], s80 offen offset:1024 lds\n"
      "v_mfma_f32_16x16x32_bf16 v[60:63], a[124:127], v[140:143], v[60:63]\n"
      "v_exp_f32 v203, v203\n"
      "v_mfma_f32_16x16x32_bf16 v[28:31], a[124:127], v[132:135], 0\n"
      "v_cvt_pk_bf16_f32 v183, v202, v203\n"
      "s_waitcnt lgkmcnt(11)\n"
      "v_mfma_f32_16x16x32_bf16 v[28:31], a[120:123], v[128:131], v[28:31]\n"
      "v_exp_f32 v204, v204\n"
      "v_mfma_f32_16x16x32_bf16 v[192:195], a[64:67], a[0:3], 0\n"
      "ds_read_b128 a[80:83], v209 offset:256\n"
      "ds_read2_b32 v[224:225], v222 offset0:0 offset1:8\n"
      "ds_read2_b32 v[226:227], v222 offset0:16 offset1:24\n"
      "v_mfma_f32_16x16x32_bf16 v[192:195], a[68:71], a[4:7], v[192:195]\n"
      "v_exp_f32 v205, v205\n"
      "v_mfma_f32_16x16x32_bf16 v[192:195], a[72:75], a[8:11], v[192:195]\n"
      "ds_read_b64_tr_b16 a[112:113], v210 offset:16896\n"
      "v_cvt_pk_bf16_f32 v190, v204, v205\n"
      "v_mfma_f32_16x16x32_bf16 v[192:195], a[76:79], a[12:15], v[192:195]\n"
      "v_exp_f32 v206, v206\n"
      "v_mfma_f32_16x16x32_bf16 v[196:199], a[76:79], a[28:31], 0\n"
      "ds_read_b128 a[84:87], v209 offset:768\n"
      "buffer_load_dwordx4 a[216:219], v246, s[48:51], 0 offen offset:128\n"
      "v_mfma_f32_16x16x32_bf16 v[196:199], a[72:75], a[24:27], v[196:199]\n"
      "v_exp_f32 v207, v207\n"
      "v_mfma_f32_16x16x32_bf16 v[196:199], a[68:71], a[20:23], v[196:199]\n"
      "ds_read_b64_tr_b16 a[114:115], v211 offset:17152\n"
      "v_cvt_pk_bf16_f32 v191, v206, v207\n"
      "v_mfma_f32_16x16x32_bf16 v[196:199], a[64:67], a[16:19], v[196:199]\n"
      "v_exp_f32 v192, v192\n"
      "v_mfma_f32_16x16x32_bf16 v[200:203], a[64:67], a[32:35], 0\n"
      "ds_read_b128 a[88:91], v209 offset:1280\n"
      "ds_read_b128 v[236:239], v223\n"
      "v_mfma_f32_16x16x32_bf16 v[200:203], a[68:71], a[36:39], v[200:203]\n"
      "v_exp_f32 v193, v193\n"
      "v_mfma_f32_16x16x32_bf16 v[200:203], a[72:75], a[40:43], v[200:203]\n"
      "ds_read_b64_tr_b16 a[116:117], v210 offset:25088\n"
      "v_cvt_pk_bf16_f32 v128, v192, v193\n"
      "v_mfma_f32_16x16x32_bf16 v[200:203], a[76:79], a[44:47], v[200:203]\n"
      "v_exp_f32 v194, v194\n"
      "v_mfma_f32_16x16x32_bf16 v[204:207], a[76:79], a[60:63], 0\n"
      "ds_read_b128 a[92:95], v209 offset:1792\n"
      "buffer_load_dwordx4 a[220:223], v246, s[48:51], 0 offen offset:192\n"
      "s_waitcnt lgkmcnt(13)\n"
      "v_mfma_f32_16x16x32_bf16 v[204:207], a[72:75], a[56:59], v[204:207]\n"
      "v_exp_f32 v195, v195\n"
      "v_mfma_f32_16x16x32_bf16 v[204:207], a[68:71], a[52:55], v[204:207]\n"
      "ds_read_b64_tr_b16 a[118:119], v211 offset:25344\n"
      "v_cvt_pk_bf16_f32 v129, v194, v195\n"
      "v_mfma_f32_16x16x32_bf16 v[204:207], a[64:67], a[48:51], v[204:207]\n"
      "v_exp_f32 v196, v196\n"
      "v_mfma_f32_16x16x32_bf16 v[0:3], a[96:99], v[160:163], v[0:3]\n"
      "ds_read_b64_tr_b16 a[120:121], v212 offset:16896\n"
      "v_add_u32 v222, 0x80, v222\n"
      "s_waitcnt lgkmcnt(9)\n"
      "v_mfma_f32_16x16x32_bf16 v[0:3], a[100:103], v[164:167], v[0:3]\n"
      "v_exp_f32 v197, v197\n"
      "v_mfma_f32_16x16x32_bf16 v[32:35], a[100:103], v[172:175], v[32:35]\n"
      "ds_read_b64_tr_b16 a[122:123], v213 offset:17152\n"
      "v_cvt_pk_bf16_f32 v136, v196, v197\n"
      "v_mfma_f32_16x16x32_bf16 v[32:35], a[96:99], v[168:171], v[32:35]\n"
      "v_exp_f32 v198, v198\n"
      "v_mfma_f32_16x16x32_bf16 v[64:67], a[96:99], v[176:179], v[64:67]\n"
      "ds_read_b64_tr_b16 a[124:125], v212 offset:25088\n"
      "v_add_u32 v223, 64, v223\n"
      "v_mad_u32_u16 v224, v224, s91, v232\n"
      "v_mfma_f32_16x16x32_bf16 v[64:67], a[100:103], v[180:183], v[64:67]\n"
      "v_exp_f32 v199, v199\n"
      "v_mfma_f32_16x16x32_bf16 v[96:99], a[100:103], v[188:191], v[96:99]\n"
      "ds_read_b64_tr_b16 a[126:127], v213 offset:25344\n"
      "v_cvt_pk_bf16_f32 v137, v198, v199\n"
      "v_mfma_f32_16x16x32_bf16 v[96:99], a[96:99], v[184:187], v[96:99]\n"
      "v_exp_f32 v200, v200\n"
      "v_mfma_f32_16x16x32_bf16 v[100:103], a[104:107], v[184:187], v[100:103]\n"
      "buffer_load_dwordx4 v216, s[40:43], s80 offen offset:2048 lds\n"
      "v_mad_u32_u16 v225, v225, s91, v233\n"
      "v_mfma_f32_16x16x32_bf16 v[100:103], a[108:111], v[188:191], v[100:103]\n"
      "v_exp_f32 v201, v201\n"
      "v_mfma_f32_16x16x32_bf16 v[68:71], a[108:111], v[180:183], v[68:71]\n"
      "v_cvt_pk_bf16_f32 v144, v200, v201\n"
      "v_mad_u32_u16 v226, v226, s91, v232\n"
      "v_mfma_f32_16x16x32_bf16 v[68:71], a[104:107], v[176:179], v[68:71]\n"
      "v_exp_f32 v202, v202\n"
      "v_mfma_f32_16x16x32_bf16 v[36:39], a[104:107], v[168:171], v[36:39]\n"
      "buffer_load_dwordx4 v217, s[40:43], s80 offen offset:3072 lds\n"
      "v_mad_u32_u16 v227, v227, s91, v233\n"
      "v_mfma_f32_16x16x32_bf16 v[36:39], a[108:111], v[172:175], v[36:39]\n"
      "v_exp_f32 v203, v203\n"
      "v_mfma_f32_16x16x32_bf16 v[4:7], a[108:111], v[164:167], v[4:7]\n"
      "v_cvt_pk_bf16_f32 v145, v202, v203\n"
      "s_waitcnt lgkmcnt(5)\n"
      "s_add_u32 m0, s90, 0\n"
      "v_mfma_f32_16x16x32_bf16 v[4:7], a[104:107], v[160:163], v[4:7]\n"
      "v_exp_f32 v204, v204\n"
      "v_mfma_f32_16x16x32_bf16 v[192:195], a[80:83], a[0:3], 0\n"
      "ds_read_b128 a[64:67], v208 offset:8192\n"
      "buffer_load_dwordx4 v224, s[40:43], 0 offen offset:0 lds\n"
      "v_and_b32 v236, v234, v236\n"
      "v_mfma_f32_16x16x32_bf16 v[192:195], a[84:87], a[4:7], v[192:195]\n"
      "v_exp_f32 v205, v205\n"
      "v_mfma_f32_16x16x32_bf16 v[192:195], a[88:91], a[8:11], v[192:195]\n"
      "ds_read_b64_tr_b16 a[96:97], v210 offset:17408\n"
      "v_cvt_pk_bf16_f32 v152, v204, v205\n"
      "v_mfma_f32_16x16x32_bf16 v[192:195], a[92:95], a[12:15], v[192:195]\n"
      "v_exp_f32 v206, v206\n"
      "v_mfma_f32_16x16x32_bf16 v[196:199], a[92:95], a[28:31], 0\n"
      "ds_read_b128 a[68:71], v208 offset:8704\n"
      "v_and_b32 v237, v234, v237\n"
      "v_mfma_f32_16x16x32_bf16 v[196:199], a[88:91], a[24:27], v[196:199]\n"
      "v_exp_f32 v207, v207\n"
      "v_mfma_f32_16x16x32_bf16 v[196:199], a[84:87], a[20:23], v[196:199]\n"
      "ds_read_b64_tr_b16 a[98:99], v211 offset:17664\n"
      "v_cvt_pk_bf16_f32 v153, v206, v207\n"
      "v_mfma_f32_16x16x32_bf16 v[196:199], a[80:83], a[16:19], v[196:199]\n"
      "v_exp_f32 v192, v192\n"
      "v_mfma_f32_16x16x32_bf16 v[200:203], a[80:83], a[32:35], 0\n"
      "ds_read_b128 a[72:75], v208 offset:9216\n"
      "buffer_load_dwordx4 v225, s[40:43], 0 offen offset:2048 lds\n"
      "v_and_b32 v238, v234, v238\n"
      "v_mfma_f32_16x16x32_bf16 v[200:203], a[84:87], a[36:39], v[200:203]\n"
      "v_exp_f32 v193, v193\n"
      "v_mfma_f32_16x16x32_bf16 v[200:203], a[88:91], a[40:43], v[200:203]\n"
      "ds_read_b64_tr_b16 a[100:101], v210 offset:25600\n"
      "v_cvt_pk_bf16_f32 v130, v192, v193\n"
      "v_mfma_f32_16x16x32_bf16 v[200:203], a[92:95], a[44:47], v[200:203]\n"
      "v_exp_f32 v194, v194\n"
      "v_mfma_f32_16x16x32_bf16 v[204:207], a[92:95], a[60:63], 0\n"
      "ds_read_b128 a[76:79], v208 offset:9728\n"
      "s_waitcnt lgkmcnt(7)\n"
      "s_add_u32 m0, s90, 4096\n"
      "v_mfma_f32_16x16x32_bf16 v[204:207], a[88:91], a[56:59], v[204:207]\n"
      "v_exp_f32 v195, v195\n"
      "v_mfma_f32_16x16x32_bf16 v[204:207], a[84:87], a[52:55], v[204:207]\n"
      "ds_read_b64_tr_b16 a[102:103], v211 offset:25856\n"
      "v_cvt_pk_bf16_f32 v131, v194, v195\n"
      "v_mfma_f32_16x16x32_bf16 v[204:207], a[80:83], a[48:51], v[204:207]\n"
      "v_exp_f32 v196, v196\n"
      "v_mfma_f32_16x16x32_bf16 v[8:11], a[112:115], v[160:163], v[8:11]\n"
      "ds_read_b64_tr_b16 a[104:105], v212 offset:17408\n"
      "buffer_load_dwordx4 v226, s[40:43], 0 offen offset:0 lds\n"
      "v_and_b32 v239, v234, v239\n"
      "v_mfma_f32_16x16x32_bf16 v[8:11], a[116:119], v[164:167], v[8:11]\n"
      "v_exp_f32 v197, v197\n"
      "v_mfma_f32_16x16x32_bf16 v[40:43], a[116:119], v[172:175], v[40:43]\n"
      "ds_read_b64_tr_b16 a[106:107], v213 offset:17664\n"
      "v_cvt_pk_bf16_f32 v138, v196, v197\n"
      "v_mfma_f32_16x16x32_bf16 v[40:43], a[112:115], v[168:171], v[40:43]\n"
      "v_exp_f32 v198, v198\n"
      "v_mfma_f32_16x16x32_bf16 v[72:75], a[112:115], v[176:179], v[72:75]\n"
      "ds_read_b64_tr_b16 a[108:109], v212 offset:25600\n"
      "v_mfma_f32_16x16x32_bf16 v[72:75], a[116:119], v[180:183], v[72:75]\n"
      "v_exp_f32 v199, v199\n"
      "v_mfma_f32_16x16x32_bf16 v[104:107], a[116:119], v[188:191], v[104:107]\n"
      "ds_read_b64_tr_b16 a[110:111], v213 offset:25856\n"
      "v_cvt_pk_bf16_f32 v139, v198, v199\n"
      "v_mfma_f32_16x16x32_bf16 v[104:107], a[112:115], v[184:187], v[104:107]\n"
      "v_exp_f32 v200, v200\n"
      "v_mfma_f32_16x16x32_bf16 v[108:111], a[120:123], v[184:187], v[108:111]\n"
      "buffer_load_dwordx4 v227, s[40:43], 0 offen offset:2048 lds\n"
      "s_add_u32 s80, s80, 0x4000\n"
      "v_mfma_f32_16x16x32_bf16 v[108:111], a[124:127], v[188:191], v[108:111]\n"
      "v_exp_f32 v201, v201\n"
      "v_mfma_f32_16x16x32_bf16 v[76:79], a[124:127], v[180:183], v[76:79]\n"
      "v_cvt_pk_bf16_f32 v146, v200, v201\n"
      "s_add_u32 m0, s90, 896\n"
      "v_mfma_f32_16x16x32_bf16 v[76:79], a[120:123], v[176:179], v[76:79]\n"
      "v_exp_f32 v202, v202\n"
      "v_mfma_f32_16x16x32_bf16 v[44:47], a[120:123], v[168:171], v[44:47]\n"
      "buffer_load_dwordx4 v224, s[40:43], 0 offen offset:128 lds\n"
      "v_mfma_f32_16x16x32_bf16 v[44:47], a[124:127], v[172:175], v[44:47]\n"
      "v_exp_f32 v203, v203\n"
      "v_mfma_f32_16x16x32_bf16 v[12:15], a[124:127], v[164:167], v[12:15]\n"
      "v_cvt_pk_bf16_f32 v147, v202, v203\n"
      "s_waitcnt lgkmcnt(5)\n"
      "v_mfma_f32_16x16x32_bf16 v[12:15], a[120:123], v[160:163], v[12:15]\n"
      "v_exp_f32 v204, v204\n"
      "v_mfma_f32_16x16x32_bf16 v[192:195], a[64:67], a[0:3], 0\n"
      "ds_read_b128 a[80:83], v209 offset:8448\n"
      "v_mfma_f32_16x16x32_bf16 v[192:195], a[68:71], a[4:7], v[192:195]\n"
      "v_exp_f32 v205, v205\n"
      "v_mfma_f32_16x16x32_bf16 v[192:195], a[72:75], a[8:11], v[192:195]\n"
      "ds_read_b64_tr_b16 a[112:113], v210 offset:17920\n"
      "v_cvt_pk_bf16_f32 v154, v204, v205\n"
      "v_mfma_f32_16x16x32_bf16 v[192:195], a[76:79], a[12:15], v[192:195]\n"
      "v_exp_f32 v206, v206\n"
      "v_mfma_f32_16x16x32_bf16 v[196:199], a[76:79], a[28:31], 0\n"
      "ds_read_b128 a[84:87], v209 offset:8960\n"
      "buffer_load_dwordx4 v225, s[40:43], 0 offen offset:2176 lds\n"
      "v_mfma_f32_16x16x32_bf16 v[196:199], a[72:75], a[24:27], v[196:199]\n"
      "v_exp_f32 v207, v207\n"
      "v_mfma_f32_16x16x32_bf16 v[196:199], a[68:71], a[20:23], v[196:199]\n"
      "ds_read_b64_tr_b16 a[114:115], v211 offset:18176\n"
      "v_cvt_pk_bf16_f32 v155, v206, v207\n"
      "v_mfma_f32_16x16x32_bf16 v[196:199], a[64:67], a[16:19], v[196:199]\n"
      "v_exp_f32 v192, v192\n"
      "v_mfma_f32_16x16x32_bf16 v[200:203], a[64:67], a[32:35], 0\n"
      "ds_read_b128 a[88:91], v209 offset:9472\n"
      "s_add_u32 m0, s90, 4992\n"
      "v_mfma_f32_16x16x32_bf16 v[200:203], a[68:71], a[36:39], v[200:203]\n"
      "v_exp_f32 v193, v193\n"
      "v_mfma_f32_16x16x32_bf16 v[200:203], a[72:75], a[40:43], v[200:203]\n"
      "ds_read_b64_tr_b16 a[116:117], v210 offset:26112\n"
      "v_cvt_pk_bf16_f32 v132, v192, v193\n"
      "v_mfma_f32_16x16x32_bf16 v[200:203], a[76:79], a[44:47], v[200:203]\n"
      "v_exp_f32 v194, v194\n"
      "v_mfma_f32_16x16x32_bf16 v[204:207], a[76:79], a[60:63], 0\n"
      "ds_read_b128 a[92:95], v209 offset:9984\n"
      "buffer_load_dwordx4 v226, s[40:43], 0 offen offset:128 lds\n"
      "s_waitcnt lgkmcnt(7)\n"
      "v_mfma_f32_16x16x32_bf16 v[204:207], a[72:75], a[56:59], v[204:207]\n"
      "v_exp_f32 v195, v195\n"
      "v_mfma_f32_16x16x32_bf16 v[204:207], a[68:71], a[52:55], v[204:207]\n"
      "ds_read_b64_tr_b16 a[118:119], v211 offset:26368\n"
      "v_cvt_pk_bf16_f32 v133, v194, v195\n"
      "v_mfma_f32_16x16x32_bf16 v[204:207], a[64:67], a[48:51], v[204:207]\n"
      "v_exp_f32 v196, v196\n"
      "v_mfma_f32_16x16x32_bf16 v[16:19], a[96:99], v[160:163], v[16:19]\n"
      "ds_read_b64_tr_b16 a[120:121], v212 offset:17920\n"
      "v_mfma_f32_16x16x32_bf16 v[16:19], a[100:103], v[164:167], v[16:19]\n"
      "v_exp_f32 v197, v197\n"
      "v_mfma_f32_16x16x32_bf16 v[48:51], a[100:103], v[172:175], v[48:51]\n"
      "ds_read_b64_tr_b16 a[122:123], v213 offset:18176\n"
      "v_cvt_pk_bf16_f32 v140, v196, v197\n"
      "v_mfma_f32_16x16x32_bf16 v[48:51], a[96:99], v[168:171], v[48:51]\n"
      "v_exp_f32 v198, v198\n"
      "v_mfma_f32_16x16x32_bf16 v[80:83], a[96:99], v[176:179], v[80:83]\n"
      "ds_read_b64_tr_b16 a[124:125], v212 offset:26112\n"
      "buffer_load_dwordx4 v227, s[40:43], 0 offen offset:2176 lds\n"
      "v_mfma_f32_16x16x32_bf16 v[80:83], a[100:103], v[180:183], v[80:83]\n"
      "v_exp_f32 v199, v199\n"
      "v_mfma_f32_16x16x32_bf16 v[112:115], a[100:103], v[188:191], v[112:115]\n"
      "ds_read_b64_tr_b16 a[126:127], v213 offset:26368\n"
      "v_cvt_pk_bf16_f32 v141, v198, v199\n"
      "v_mfma_f32_16x16x32_bf16 v[112:115], a[96:99], v[184:187], v[112:115]\n"
      "v_exp_f32 v200, v200\n"
      "v_mfma_f32_16x16x32_bf16 v[116:119], a[104:107], v[184:187], v[116:119]\n"
      "v_add_u32 v210, s81, v241\n"
      "v_xor_b32 v211, 16, v210\n"
      "v_mfma_f32_16x16x32_bf16 v[116:119], a[108:111], v[188:191], v[116:119]\n"
      "v_exp_f32 v201, v201\n"
      "v_mfma_f32_16x16x32_bf16 v[84:87], a[108:111], v[180:183], v[84:87]\n"
      "v_cvt_pk_bf16_f32 v148, v200, v201\n"
      "v_xor_b32 v212, 32, v210\n"
      "v_mfma_f32_16x16x32_bf16 v[84:87], a[104:107], v[176:179], v[84:87]\n"
      "v_exp_f32 v202, v202\n"
      "v_mfma_f32_16x16x32_bf16 v[52:55], a[104:107], v[168:171], v[52:55]\n"
      "ds_read2_b32 v[224:225], v222 offset0:0 offset1:8\n"
      "ds_read2_b32 v[226:227], v222 offset0:16 offset1:24\n"
      "v_mfma_f32_16x16x32_bf16 v[52:55], a[108:111], v[172:175], v[52:55]\n"
      "v_exp_f32 v203, v203\n"
      "v_mfma_f32_16x16x32_bf16 v[20:23], a[108:111], v[164:167], v[20:23]\n"
      "v_cvt_pk_bf16_f32 v149, v202, v203\n"
      "s_waitcnt vmcnt(20) lgkmcnt(2)\n"
      "s_barrier\n"
      "v_mfma_f32_16x16x32_bf16 v[20:23], a[104:107], v[160:163], v[20:23]\n"
      "v_exp_f32 v204, v204\n"
      "v_mfma_f32_16x16x32_bf16 v[192:195], a[80:83], a[0:3], 0\n"
      "ds_read_b128 a[64:67], v208 offset:16384\n"
      "v_xor_b32 v213, 48, v210\n"
      "v_mfma_f32_16x16x32_bf16 v[192:195], a[84:87], a[4:7], v[192:195]\n"
      "v_exp_f32 v205, v205\n"
      "v_mfma_f32_16x16x32_bf16 v[192:195], a[88:91], a[8:11], v[192:195]\n"
      "ds_read_b64_tr_b16 a[96:97], v210 offset:0\n"
      "v_cvt_pk_bf16_f32 v156, v204, v205\n"
      "v_mfma_f32_16x16x32_bf16 v[192:195], a[92:95], a[12:15], v[192:195]\n"
      "v_exp_f32 v206, v206\n"
      "v_mfma_f32_16x16x32_bf16 v[196:199], a[92:95], a[28:31], 0\n"
      "ds_read_b128 a[68:71], v208 offset:16896\n"
      "v_lshlrev_b32 v228, v235, v236\n"
      "v_mfma_f32_16x16x32_bf16 v[196:199], a[88:91], a[24:27], v[196:199]\n"
      "v_exp_f32 v207, v207\n"
      "v_mfma_f32_16x16x32_bf16 v[196:199], a[84:87], a[20:23], v[196:199]\n"
      "ds_read_b64_tr_b16 a[98:99], v211 offset:256\n"
      "v_cvt_pk_bf16_f32 v157, v206, v207\n"
      "v_mfma_f32_16x16x32_bf16 v[196:199], a[80:83], a[16:19], v[196:199]\n"
      "v_exp_f32 v192, v192\n"
      "v_mfma_f32_16x16x32_bf16 v[200:203], a[80:83], a[32:35], 0\n"
      "ds_read_b128 a[72:75], v208 offset:17408\n"
      "v_lshlrev_b32 v229, v235, v237\n"
      "v_mfma_f32_16x16x32_bf16 v[200:203], a[84:87], a[36:39], v[200:203]\n"
      "v_exp_f32 v193, v193\n"
      "v_mfma_f32_16x16x32_bf16 v[200:203], a[88:91], a[40:43], v[200:203]\n"
      "ds_read_b64_tr_b16 a[100:101], v210 offset:8192\n"
      "v_cvt_pk_bf16_f32 v134, v192, v193\n"
      "v_mfma_f32_16x16x32_bf16 v[200:203], a[92:95], a[44:47], v[200:203]\n"
      "v_exp_f32 v194, v194\n"
      "v_mfma_f32_16x16x32_bf16 v[204:207], a[92:95], a[60:63], 0\n"
      "ds_read_b128 a[76:79], v208 offset:17920\n"
      "v_lshlrev_b32 v230, v235, v238\n"
      "v_mfma_f32_16x16x32_bf16 v[204:207], a[88:91], a[56:59], v[204:207]\n"
      "v_exp_f32 v195, v195\n"
      "v_mfma_f32_16x16x32_bf16 v[204:207], a[84:87], a[52:55], v[204:207]\n"
      "ds_read_b64_tr_b16 a[102:103], v211 offset:8448\n"
      "v_cvt_pk_bf16_f32 v135, v194, v195\n"
      "v_mfma_f32_16x16x32_bf16 v[204:207], a[80:83], a[48:51], v[204:207]\n"
      "v_exp_f32 v196, v196\n"
      "v_mfma_f32_16x16x32_bf16 v[24:27], a[112:115], v[160:163], v[24:27]\n"
      "ds_read_b64_tr_b16 a[104:105], v212 offset:0\n"
      "v_lshlrev_b32 v231, v235, v239\n"
      "v_mfma_f32_16x16x32_bf16 v[24:27], a[116:119], v[164:167], v[24:27]\n"
      "v_exp_f32 v197, v197\n"
      "v_mfma_f32_16x16x32_bf16 v[56:59], a[116:119], v[172:175], v[56:59]\n"
      "ds_read_b64_tr_b16 a[106:107], v213 offset:256\n"
      "v_cvt_pk_bf16_f32 v142, v196, v197\n"
      "v_mfma_f32_16x16x32_bf16 v[56:59], a[112:115], v[168:171], v[56:59]\n"
      "v_exp_f32 v198, v198\n"
      "v_mfma_f32_16x16x32_bf16 v[88:91], a[112:115], v[176:179], v[88:91]\n"
      "ds_read_b64_tr_b16 a[108:109], v212 offset:8192\n"
      "s_mov_b32 m0, s87\n"
      "v_mfma_f32_16x16x32_bf16 v[88:91], a[116:119], v[180:183], v[88:91]\n"
      "v_exp_f32 v199, v199\n"
      "v_mfma_f32_16x16x32_bf16 v[120:123], a[116:119], v[188:191], v[120:123]\n"
      "ds_read_b64_tr_b16 a[110:111], v213 offset:8448\n"
      "v_cvt_pk_bf16_f32 v143, v198, v199\n"
      "v_mfma_f32_16x16x32_bf16 v[120:123], a[112:115], v[184:187], v[120:123]\n"
      "v_exp_f32 v200, v200\n"
      "v_mfma_f32_16x16x32_bf16 v[124:127], a[120:123], v[184:187], v[124:127]\n"
      "buffer_load_dwordx4 v214, s[40:43], s80 offen offset:0 lds\n"
      "s_mov_b32 s87, s81\n"
      "v_mfma_f32_16x16x32_bf16 v[124:127], a[124:127], v[188:191], v[124:127]\n"
      "v_exp_f32 v201, v201\n"
      "v_mfma_f32_16x16x32_bf16 v[92:95], a[124:127], v[180:183], v[92:95]\n"
      "v_cvt_pk_bf16_f32 v150, v200, v201\n"
      "s_mov_b32 s81, s86\n"
      "v_mfma_f32_16x16x32_bf16 v[92:95], a[120:123], v[176:179], v[92:95]\n"
      "v_exp_f32 v202, v202\n"
      "v_mfma_f32_16x16x32_bf16 v[60:63], a[120:123], v[168:171], v[60:63]\n"
      "buffer_load_dwordx4 v215, s[40:43], s80 offen offset:1024 lds\n"
      "s_mov_b32 s86, m0\n"
      "v_mfma_f32_16x16x32_bf16 v[60:63], a[124:127], v[172:175], v[60:63]\n"
      "v_exp_f32 v203, v203\n"
      "v_mfma_f32_16x16x32_bf16 v[28:31], a[124:127], v[164:167], v[28:31]\n"
      "v_cvt_pk_bf16_f32 v151, v202, v203\n"
      "s_waitcnt vmcnt(6) lgkmcnt(5)\n"
      "v_mfma_f32_16x16x32_bf16 v[28:31], a[120:123], v[160:163], v[28:31]\n"
      "v_exp_f32 v204, v204\n"
      ".p2align 5\n"
      "1:\n"
      "v_mfma_f32_16x16x32_bf16 v[192:195], a[64:67], a[0:3], 0\n"
      "ds_read_b128 a[80:83], v209 offset:16640\n"
      "ds_read_b64_tr_b16 a[160:161], v218 offset:0\n"
      "v_mfma_f32_16x16x32_bf16 v[192:195], a[68:71], a[4:7], v[192:195]\n"
      "v_exp_f32 v205, v205\n"
      "v_mfma_f32_16x16x32_bf16 v[192:195], a[72:75], a[8:11], v[192:195]\n"
      "ds_read_b64_tr_b16 a[112:113], v210 offset:512\n"
      "v_cvt_pk_bf16_f32 v158, v204, v205\n"
      "v_mfma_f32_16x16x32_bf16 v[192:195], a[76:79], a[12:15], v[192:195]\n"
      "v_exp_f32 v206, v206\n"
      "v_mfma_f32_16x16x32_bf16 v[196:199], a[76:79], a[28:31], 0\n"
      "ds_read_b128 a[84:87], v209 offset:17152\n"
      "ds_read_b64_tr_b16 a[162:163], v219 offset:256\n"
      "v_mfma_f32_16x16x32_bf16 v[196:199], a[72:75], a[24:27], v[196:199]\n"
      "v_exp_f32 v207, v207\n"
      "v_mfma_f32_16x16x32_bf16 v[196:199], a[68:71], a[20:23], v[196:199]\n"
      "ds_read_b64_tr_b16 a[114:115], v211 offset:768\n"
      "v_cvt_pk_bf16_f32 v159, v206, v207\n"
      "v_mfma_f32_16x16x32_bf16 v[196:199], a[64:67], a[16:19], v[196:199]\n"
      "v_exp_f32 v192, v192\n"
      "v_mfma_f32_16x16x32_bf16 v[200:203], a[64:67], a[32:35], 0\n"
      "ds_read_b128 a[88:91], v209 offset:17664\n"
      "ds_read_b64_tr_b16 a[164:165], v220 offset:0\n"
      "v_mfma_f32_16x16x32_bf16 v[200:203], a[68:71], a[36:39], v[200:203]\n"
      "v_exp_f32 v193, v193\n"
      "v_mfma_f32_16x16x32_bf16 v[200:203], a[72:75], a[40:43], v[200:203]\n"
      "ds_read_b64_tr_b16 a[116:117], v210 offset:8704\n"
      "v_cvt_pk_bf16_f32 v160, v192, v193\n"
      "v_mfma_f32_16x16x32_bf16 v[200:203], a[76:79], a[44:47], v[200:203]\n"
      "v_exp_f32 v194, v194\n"
      "v_mfma_f32_16x16x32_bf16 v[204:207], a[76:79], a[60:63], 0\n"
      "ds_read_b128 a[92:95], v209 offset:18176\n"
      "s_waitcnt lgkmcnt(10)\n"
      "v_mfma_f32_16x16x32_bf16 v[204:207], a[72:75], a[56:59], v[204:207]\n"
      "v_exp_f32 v195, v195\n"
      "v_mfma_f32_16x16x32_bf16 v[204:207], a[68:71], a[52:55], v[204:207]\n"
      "ds_read_b64_tr_b16 a[118:119], v211 offset:8960\n"
      "v_cvt_pk_bf16_f32 v161, v194, v195\n"
      "v_mfma_f32_16x16x32_bf16 v[204:207], a[64:67], a[48:51], v[204:207]\n"
      "v_exp_f32 v196, v196\n"
      "v_mfma_f32_16x16x32_bf16 v[0:3], a[96:99], v[128:131], v[0:3]\n"
      "ds_read_b64_tr_b16 a[120:121], v212 offset:512\n"
      "ds_read_b64_tr_b16 a[166:167], v221 offset:256\n"
      "v_mfma_f32_16x16x32_bf16 v[0:3], a[100:103], v[132:135], v[0:3]\n"
      "v_exp_f32 v197, v197\n"
      "v_mfma_f32_16x16x32_bf16 v[32:35], a[100:103], v[140:143], v[32:35]\n"
      "ds_read_b64_tr_b16 a[122:123], v213 offset:768\n"
      "v_cvt_pk_bf16_f32 v168, v196, v197\n"
      "v_mfma_f32_16x16x32_bf16 v[32:35], a[96:99], v[136:139], v[32:35]\n"
      "v_exp_f32 v198, v198\n"
      "v_mfma_f32_16x16x32_bf16 v[64:67], a[96:99], v[144:147], v[64:67]\n"
      "ds_read_b64_tr_b16 a[124:125], v212 offset:8704\n"
      "ds_read_b64_tr_b16 a[168:169], v218 offset:512\n"
      "v_mfma_f32_16x16x32_bf16 v[64:67], a[100:103], v[148:151], v[64:67]\n"
      "v_exp_f32 v199, v199\n"
      "v_mfma_f32_16x16x32_bf16 v[96:99], a[100:103], v[156:159], v[96:99]\n"
      "ds_read_b64_tr_b16 a[126:127], v213 offset:8960\n"
      "v_cvt_pk_bf16_f32 v169, v198, v199\n"
      "v_mfma_f32_16x16x32_bf16 v[96:99], a[96:99], v[152:155], v[96:99]\n"
      "v_exp_f32 v200, v200\n"
      "v_mfma_f32_16x16x32_bf16 v[100:103], a[104:107], v[152:155], v[100:103]\n"
      "buffer_load_dwordx4 v216, s[40:43], s80 offen offset:2048 lds\n"
      "ds_read_b64_tr_b16 a[170:171], v219 offset:768\n"
      "v_mfma_f32_16x16x32_bf16 v[100:103], a[108:111], v[156:159], v[100:103]\n"
      "v_exp_f32 v201, v201\n"
      "v_mfma_f32_16x16x32_bf16 v[68:71], a[108:111], v[148:151], v[68:71]\n"
      "ds_read_b64_tr_b16 a[172:173], v220 offset:512\n"
      "v_cvt_pk_bf16_f32 v176, v200, v201\n"
      "v_mfma_f32_16x16x32_bf16 v[68:71], a[104:107], v[144:147], v[68:71]\n"
      "v_exp_f32 v202, v202\n"
      "v_mfma_f32_16x16x32_bf16 v[36:39], a[104:107], v[136:139], v[36:39]\n"
      "buffer_load_dwordx4 v217, s[40:43], s80 offen offset:3072 lds\n"
      "ds_read_b64_tr_b16 a[174:175], v221 offset:768\n"
      "v_mfma_f32_16x16x32_bf16 v[36:39], a[108:111], v[140:143], v[36:39]\n"
      "v_exp_f32 v203, v203\n"
      "v_mfma_f32_16x16x32_bf16 v[4:7], a[108:111], v[132:135], v[4:7]\n"
      "v_cvt_pk_bf16_f32 v177, v202, v203\n"
      "s_waitcnt lgkmcnt(10)\n"
      "v_mfma_f32_16x16x32_bf16 v[4:7], a[104:107], v[128:131], v[4:7]\n"
      "v_exp_f32 v204, v204\n"
      "v_mfma_f32_16x16x32_bf16 v[192:195], a[80:83], a[0:3], 0\n"
      "ds_read_b128 a[64:67], v208 offset:24576\n"
      "v_mad_u32_u16 v224, v224, s91, v232\n"
      "v_mfma_f32_16x16x32_bf16 v[192:195], a[84:87], a[4:7], v[192:195]\n"
      "v_exp_f32 v205, v205\n"
      "v_mfma_f32_16x16x32_bf16 v[192:195], a[88:91], a[8:11], v[192:195]\n"
      "ds_read_b64_tr_b16 a[96:97], v210 offset:1024\n"
      "v_cvt_pk_bf16_f32 v184, v204, v205\n"
      "v_mfma_f32_16x16x32_bf16 v[192:195], a[92:95], a[12:15], v[192:195]\n"
      "v_exp_f32 v206, v206\n"
      "v_mfma_f32_16x16x32_bf16 v[196:199], a[92:95], a[28:31], 0\n"
      "ds_read_b128 a[68:71], v208 offset:25088\n"
      "v_mad_u32_u16 v225, v225, s91, v233\n"
      "v_mfma_f32_16x16x32_bf16 v[196:199], a[88:91], a[24:27], v[196:199]\n"
      "v_exp_f32 v207, v207\n"
      "v_mfma_f32_16x16x32_bf16 v[196:199], a[84:87], a[20:23], v[196:199]\n"
      "ds_read_b64_tr_b16 a[98:99], v211 offset:1280\n"
      "v_cvt_pk_bf16_f32 v185, v206, v207\n"
      "v_mfma_f32_16x16x32_bf16 v[196:199], a[80:83], a[16:19], v[196:199]\n"
      "v_exp_f32 v192, v192\n"
      "v_mfma_f32_16x16x32_bf16 v[200:203], a[80:83], a[32:35], 0\n"
      "ds_read_b128 a[72:75], v208 offset:25600\n"
      "v_mad_u32_u16 v226, v226, s91, v232\n"
      "v_mfma_f32_16x16x32_bf16 v[200:203], a[84:87], a[36:39], v[200:203]\n"
      "v_exp_f32 v193, v193\n"
      "v_mfma_f32_16x16x32_bf16 v[200:203], a[88:91], a[40:43], v[200:203]\n"
      "ds_read_b64_tr_b16 a[100:101], v210 offset:9216\n"
      "v_cvt_pk_bf16_f32 v162, v192, v193\n"
      "v_mfma_f32_16x16x32_bf16 v[200:203], a[92:95], a[44:47], v[200:203]\n"
      "v_exp_f32 v194, v194\n"
      "v_mfma_f32_16x16x32_bf16 v[204:207], a[92:95], a[60:63], 0\n"
      "ds_read_b128 a[76:79], v208 offset:26112\n"
      "s_waitcnt lgkmcnt(7)\n"
      "v_mfma_f32_16x16x32_bf16 v[204:207], a[88:91], a[56:59], v[204:207]\n"
      "v_exp_f32 v195, v195\n"
      "v_mfma_f32_16x16x32_bf16 v[204:207], a[84:87], a[52:55], v[204:207]\n"
      "ds_read_b64_tr_b16 a[102:103], v211 offset:9472\n"
      "v_cvt_pk_bf16_f32 v163, v194, v195\n"
      "v_mfma_f32_16x16x32_bf16 v[204:207], a[80:83], a[48:51], v[204:207]\n"
      "v_exp_f32 v196, v196\n"
      "v_mfma_f32_16x16x32_bf16 v[8:11], a[112:115], v[128:131], v[8:11]\n"
      "ds_read_b64_tr_b16 a[104:105], v212 offset:1024\n"
      "v_mad_u32_u16 v227, v227, s91, v233\n"
      "v_mfma_f32_16x16x32_bf16 v[8:11], a[116:119], v[132:135], v[8:11]\n"
      "v_exp_f32 v197, v197\n"
      "v_mfma_f32_16x16x32_bf16 v[40:43], a[116:119], v[140:143], v[40:43]\n"
      "ds_read_b64_tr_b16 a[106:107], v213 offset:1280\n"
      "v_cvt_pk_bf16_f32 v170, v196, v197\n"
      "v_mfma_f32_16x16x32_bf16 v[40:43], a[112:115], v[136:139], v[40:43]\n"
      "v_exp_f32 v198, v198\n"
      "v_mfma_f32_16x16x32_bf16 v[72:75], a[112:115], v[144:147], v[72:75]\n"
      "ds_read_b64_tr_b16 a[108:109], v212 offset:9216\n"
      "s_add_u32 m0, s90, 0\n"
      "v_mfma_f32_16x16x32_bf16 v[72:75], a[116:119], v[148:151], v[72:75]\n"
      "v_exp_f32 v199, v199\n"
      "v_mfma_f32_16x16x32_bf16 v[104:107], a[116:119], v[156:159], v[104:107]\n"
      "ds_read_b64_tr_b16 a[110:111], v213 offset:9472\n"
      "v_cvt_pk_bf16_f32 v171, v198, v199\n"
      "v_mfma_f32_16x16x32_bf16 v[104:107], a[112:115], v[152:155], v[104:107]\n"
      "v_exp_f32 v200, v200\n"
      "v_mfma_f32_16x16x32_bf16 a[128:131], a[160:163], v[228:231], a[128:131]\n"
      "buffer_load_dwordx4 v224, s[40:43], 0 offen offset:0 lds\n"
      "v_mfma_f32_16x16x32_bf16 v[108:111], a[120:123], v[152:155], v[108:111]\n"
      "s_add_u32 s80, s80, 0x4000\n"
      "v_mfma_f32_16x16x32_bf16 v[108:111], a[124:127], v[156:159], v[108:111]\n"
      "v_exp_f32 v201, v201\n"
      "v_mfma_f32_16x16x32_bf16 v[76:79], a[124:127], v[148:151], v[76:79]\n"
      "v_cvt_pk_bf16_f32 v178, v200, v201\n"
      "s_cmp_eq_u32 s80, s89\n"
      "v_mfma_f32_16x16x32_bf16 v[76:79], a[120:123], v[144:147], v[76:79]\n"
      "v_exp_f32 v202, v202\n"
      "v_mfma_f32_16x16x32_bf16 a[132:135], a[164:167], v[228:231], a[132:135]\n"
      "buffer_load_dwordx4 v225, s[40:43], 0 offen offset:2048 lds\n"
      "v_mfma_f32_16x16x32_bf16 v[44:47], a[120:123], v[136:139], v[44:47]\n"
      "s_cselect_b32 s80, s92, s80\n"
      "v_mfma_f32_16x16x32_bf16 v[44:47], a[124:127], v[140:143], v[44:47]\n"
      "v_exp_f32 v203, v203\n"
      "v_mfma_f32_16x16x32_bf16 v[12:15], a[124:127], v[132:135], v[12:15]\n"
      "v_cvt_pk_bf16_f32 v179, v202, v203\n"
      "s_waitcnt lgkmcnt(5)\n"
      "v_mfma_f32_16x16x32_bf16 v[12:15], a[120:123], v[128:131], v[12:15]\n"
      "v_exp_f32 v204, v204\n"
      "v_mfma_f32_16x16x32_bf16 v[192:195], a[64:67], a[0:3], 0\n"
      "ds_read_b128 a[80:83], v209 offset:24832\n"
      "v_add_u32 v222, 0x80, v222\n"
      "v_mfma_f32_16x16x32_bf16 v[192:195], a[68:71], a[4:7], v[192:195]\n"
      "v_exp_f32 v205, v205\n"
      "v_mfma_f32_16x16x32_bf16 v[192:195], a[72:75], a[8:11], v[192:195]\n"
      "ds_read_b64_tr_b16 a[112:113], v210 offset:1536\n"
      "v_cvt_pk_bf16_f32 v186, v204, v205\n"
      "v_mfma_f32_16x16x32_bf16 v[192:195], a[76:79], a[12:15], v[192:195]\n"
      "v_exp_f32 v206, v206\n"
      "v_mfma_f32_16x16x32_bf16 v[196:199], a[76:79], a[28:31], 0\n"
      "ds_read_b128 a[84:87], v209 offset:25344\n"
      "v_mfma_f32_16x16x32_bf16 v[196:199], a[72:75], a[24:27], v[196:199]\n"
      "v_exp_f32 v207, v207\n"
      "v_mfma_f32_16x16x32_bf16 v[196:199], a[68:71], a[20:23], v[196:199]\n"
      "ds_read_b64_tr_b16 a[114:115], v211 offset:1792\n"
      "v_cvt_pk_bf16_f32 v187, v206, v207\n"
      "v_mfma_f32_16x16x32_bf16 v[196:199], a[64:67], a[16:19], v[196:199]\n"
      "v_exp_f32 v192, v192\n"
      "v_mfma_f32_16x16x32_bf16 v[200:203], a[64:67], a[32:35], 0\n"
      "ds_read_b128 a[88:91], v209 offset:25856\n"
      "v_mfma_f32_16x16x32_bf16 v[200:203], a[68:71], a[36:39], v[200:203]\n"
      "v_exp_f32 v193, v193\n"
      "v_mfma_f32_16x16x32_bf16 v[200:203], a[72:75], a[40:43], v[200:203]\n"
      "ds_read_b64_tr_b16 a[116:117], v210 offset:9728\n"
      "v_cvt_pk_bf16_f32 v164, v192, v193\n"
      "v_mfma_f32_16x16x32_bf16 v[200:203], a[76:79], a[44:47], v[200:203]\n"
      "v_exp_f32 v194, v194\n"
      "v_mfma_f32_16x16x32_bf16 v[204:207], a[76:79], a[60:63], 0\n"
      "ds_read_b128 a[92:95], v209 offset:26368\n"
      "s_waitcnt lgkmcnt(7)\n"
      "v_mfma_f32_16x16x32_bf16 v[204:207], a[72:75], a[56:59], v[204:207]\n"
      "v_exp_f32 v195, v195\n"
      "v_mfma_f32_16x16x32_bf16 v[204:207], a[68:71], a[52:55], v[204:207]\n"
      "ds_read_b64_tr_b16 a[118:119], v211 offset:9984\n"
      "v_cvt_pk_bf16_f32 v165, v194, v195\n"
      "v_mfma_f32_16x16x32_bf16 v[204:207], a[64:67], a[48:51], v[204:207]\n"
      "v_exp_f32 v196, v196\n"
      "v_mfma_f32_16x16x32_bf16 v[16:19], a[96:99], v[128:131], v[16:19]\n"
      "ds_read_b64_tr_b16 a[120:121], v212 offset:1536\n"
      "v_add_u32 v208, s81, v240\n"
      "v_mfma_f32_16x16x32_bf16 v[16:19], a[100:103], v[132:135], v[16:19]\n"
      "v_exp_f32 v197, v197\n"
      "v_mfma_f32_16x16x32_bf16 v[48:51], a[100:103], v[140:143], v[48:51]\n"
      "ds_read_b64_tr_b16 a[122:123], v213 offset:1792\n"
      "v_cvt_pk_bf16_f32 v172, v196, v197\n"
      "v_mfma_f32_16x16x32_bf16 v[48:51], a[96:99], v[136:139], v[48:51]\n"
      "v_exp_f32 v198, v198\n"
      "v_mfma_f32_16x16x32_bf16 v[80:83], a[96:99], v[144:147], v[80:83]\n"
      "ds_read_b64_tr_b16 a[124:125], v212 offset:9728\n"
      "s_add_u32 m0, s90, 4096\n"
      "v_mfma_f32_16x16x32_bf16 v[80:83], a[100:103], v[148:151], v[80:83]\n"
      "v_exp_f32 v199, v199\n"
      "v_mfma_f32_16x16x32_bf16 v[112:115], a[100:103], v[156:159], v[112:115]\n"
      "ds_read_b64_tr_b16 a[126:127], v213 offset:9984\n"
      "v_cvt_pk_bf16_f32 v173, v198, v199\n"
      "v_mfma_f32_16x16x32_bf16 v[112:115], a[96:99], v[152:155], v[112:115]\n"
      "v_exp_f32 v200, v200\n"
      "v_mfma_f32_16x16x32_bf16 a[136:139], a[168:171], v[228:231], a[136:139]\n"
      "buffer_load_dwordx4 v226, s[40:43], 0 offen offset:0 lds\n"
      "v_mfma_f32_16x16x32_bf16 v[116:119], a[104:107], v[152:155], v[116:119]\n"
      "v_mfma_f32_16x16x32_bf16 v[116:119], a[108:111], v[156:159], v[116:119]\n"
      "v_exp_f32 v201, v201\n"
      "v_mfma_f32_16x16x32_bf16 v[84:87], a[108:111], v[148:151], v[84:87]\n"
      "v_cvt_pk_bf16_f32 v180, v200, v201\n"
      "v_xor_b32 v209, 16, v208\n"
      "v_mfma_f32_16x16x32_bf16 v[84:87], a[104:107], v[144:147], v[84:87]\n"
      "v_exp_f32 v202, v202\n"
      "v_mfma_f32_16x16x32_bf16 a[140:143], a[172:175], v[228:231], a[140:143]\n"
      "buffer_load_dwordx4 v227, s[40:43], 0 offen offset:2048 lds\n"
      "v_mfma_f32_16x16x32_bf16 v[52:55], a[104:107], v[136:139], v[52:55]\n"
      "v_mfma_f32_16x16x32_bf16 v[52:55], a[108:111], v[140:143], v[52:55]\n"
      "v_exp_f32 v203, v203\n"
      "v_mfma_f32_16x16x32_bf16 v[20:23], a[108:111], v[132:135], v[20:23]\n"
      "v_cvt_pk_bf16_f32 v181, v202, v203\n"
      "s_waitcnt lgkmcnt(5)\n"
      "v_mfma_f32_16x16x32_bf16 v[20:23], a[104:107], v[128:131], v[20:23]\n"
      "v_exp_f32 v204, v204\n"
      "v_mfma_f32_16x16x32_bf16 v[192:195], a[80:83], a[0:3], 0\n"
      "ds_read_b128 a[64:67], v208 offset:0\n"
      "ds_read_b128 v[236:239], v223\n"
      "v_mfma_f32_16x16x32_bf16 v[192:195], a[84:87], a[4:7], v[192:195]\n"
      "v_exp_f32 v205, v205\n"
      "v_mfma_f32_16x16x32_bf16 v[192:195], a[88:91], a[8:11], v[192:195]\n"
      "ds_read_b64_tr_b16 a[96:97], v210 offset:16384\n"
      "v_cvt_pk_bf16_f32 v188, v204, v205\n"
      "v_mfma_f32_16x16x32_bf16 v[192:195], a[92:95], a[12:15], v[192:195]\n"
      "v_exp_f32 v206, v206\n"
      "v_mfma_f32_16x16x32_bf16 v[196:199], a[92:95], a[28:31], 0\n"
      "ds_read_b128 a[68:71], v208 offset:512\n"
      "v_add_u32 v223, 64, v223\n"
      "v_mfma_f32_16x16x32_bf16 v[196:199], a[88:91], a[24:27], v[196:199]\n"
      "v_exp_f32 v207, v207\n"
      "v_mfma_f32_16x16x32_bf16 v[196:199], a[84:87], a[20:23], v[196:199]\n"
      "ds_read_b64_tr_b16 a[98:99], v211 offset:16640\n"
      "v_cvt_pk_bf16_f32 v189, v206, v207\n"
      "v_mfma_f32_16x16x32_bf16 v[196:199], a[80:83], a[16:19], v[196:199]\n"
      "v_exp_f32 v192, v192\n"
      "v_mfma_f32_16x16x32_bf16 v[200:203], a[80:83], a[32:35], 0\n"
      "ds_read_b128 a[72:75], v208 offset:1024\n"
      "v_mfma_f32_16x16x32_bf16 v[200:203], a[84:87], a[36:39], v[200:203]\n"
      "v_exp_f32 v193, v193\n"
      "v_mfma_f32_16x16x32_bf16 v[200:203], a[88:91], a[40:43], v[200:203]\n"
      "ds_read_b64_tr_b16 a[100:101], v210 offset:24576\n"
      "v_cvt_pk_bf16_f32 v166, v192, v193\n"
      "v_mfma_f32_16x16x32_bf16 v[200:203], a[92:95], a[44:47], v[200:203]\n"
      "v_exp_f32 v194, v194\n"
      "v_mfma_f32_16x16x32_bf16 v[204:207], a[92:95], a[60:63], 0\n"
      "ds_read_b128 a[76:79], v208 offset:1536\n"
      "s_waitcnt lgkmcnt(8)\n"
      "v_mfma_f32_16x16x32_bf16 v[204:207], a[88:91], a[56:59], v[204:207]\n"
      "v_exp_f32 v195, v195\n"
      "v_mfma_f32_16x16x32_bf16 v[204:207], a[84:87], a[52:55], v[204:207]\n"
      "ds_read_b64_tr_b16 a[102:103], v211 offset:24832\n"
      "v_cvt_pk_bf16_f32 v167, v194, v195\n"
      "v_mfma_f32_16x16x32_bf16 v[204:207], a[80:83], a[48:51], v[204:207]\n"
      "v_exp_f32 v196, v196\n"
      "v_mfma_f32_16x16x32_bf16 v[24:27], a[112:115], v[128:131], v[24:27]\n"
      "ds_read_b64_tr_b16 a[104:105], v212 offset:16384\n"
      "v_mfma_f32_16x16x32_bf16 v[24:27], a[116:119], v[132:135], v[24:27]\n"
      "v_exp_f32 v197, v197\n"
      "v_mfma_f32_16x16x32_bf16 v[56:59], a[116:119], v[140:143], v[56:59]\n"
      "ds_read_b64_tr_b16 a[106:107], v213 offset:16640\n"
      "v_cvt_pk_bf16_f32 v174, v196, v197\n"
      "v_mfma_f32_16x16x32_bf16 v[56:59], a[112:115], v[136:139], v[56:59]\n"
      "v_exp_f32 v198, v198\n"
      "v_mfma_f32_16x16x32_bf16 v[88:91], a[112:115], v[144:147], v[88:91]\n"
      "ds_read_b64_tr_b16 a[108:109], v212 offset:24576\n"
      "s_add_u32 m0, s86, 0x4000\n"
      "v_mfma_f32_16x16x32_bf16 v[88:91], a[116:119], v[148:151], v[88:91]\n"
      "v_exp_f32 v199, v199\n"
      "v_mfma_f32_16x16x32_bf16 v[120:123], a[116:119], v[156:159], v[120:123]\n"
      "ds_read_b64_tr_b16 a[110:111], v213 offset:24832\n"
      "v_cvt_pk_bf16_f32 v175, v198, v199\n"
      "v_mfma_f32_16x16x32_bf16 v[120:123], a[112:115], v[152:155], v[120:123]\n"
      "v_exp_f32 v200, v200\n"
      "v_mfma_f32_16x16x32_bf16 v[124:127], a[120:123], v[152:155], v[124:127]\n"
      "buffer_load_dwordx4 v214, s[40:43], s80 offen offset:0 lds\n"
      "v_mfma_f32_16x16x32_bf16 v[124:127], a[124:127], v[156:159], v[124:127]\n"
      "v_exp_f32 v201, v201\n"
      "v_mfma_f32_16x16x32_bf16 v[92:95], a[124:127], v[148:151], v[92:95]\n"
      "v_cvt_pk_bf16_f32 v182, v200, v201\n"
      "v_mfma_f32_16x16x32_bf16 v[92:95], a[120:123], v[144:147], v[92:95]\n"
      "v_exp_f32 v202, v202\n"
      "v_mfma_f32_16x16x32_bf16 v[60:63], a[120:123], v[136:139], v[60:63]\n"
      "buffer_load_dwordx4 v215, s[40:43], s80 offen offset:1024 lds\n"
      "v_mfma_f32_16x16x32_bf16 v[60:63], a[124:127], v[140:143], v[60:63]\n"
      "v_exp_f32 v203, v203\n"
      "v_mfma_f32_16x16x32_bf16 v[28:31], a[124:127], v[132:135], v[28:31]\n"
      "v_cvt_pk_bf16_f32 v183, v202, v203\n"
      "s_waitcnt vmcnt(10) lgkmcnt(5)\n"
      "v_mfma_f32_16x16x32_bf16 v[28:31], a[120:123], v[128:131], v[28:31]\n"
      "v_exp_f32 v204, v204\n"
      "v_mfma_f32_16x16x32_bf16 v[192:195], a[64:67], a[0:3], 0\n"
      "ds_read_b128 a[80:83], v209 offset:256\n"
      "ds_read_b64_tr_b16 a[160:161], v218 offset:1024\n"
      "v_mfma_f32_16x16x32_bf16 v[192:195], a[68:71], a[4:7], v[192:195]\n"
      "v_exp_f32 v205, v205\n"
      "v_mfma_f32_16x16x32_bf16 v[192:195], a[72:75], a[8:11], v[192:195]\n"
      "ds_read_b64_tr_b16 a[112:113], v210 offset:16896\n"
      "v_cvt_pk_bf16_f32 v190, v204, v205\n"
      "v_mfma_f32_16x16x32_bf16 v[192:195], a[76:79], a[12:15], v[192:195]\n"
      "v_exp_f32 v206, v206\n"
      "v_mfma_f32_16x16x32_bf16 v[196:199], a[76:79], a[28:31], 0\n"
      "ds_read_b128 a[84:87], v209 offset:768\n"
      "ds_read_b64_tr_b16 a[162:163], v219 offset:1280\n"
      "v_mfma_f32_16x16x32_bf16 v[196:199], a[72:75], a[24:27], v[196:199]\n"
      "v_exp_f32 v207, v207\n"
      "v_mfma_f32_16x16x32_bf16 v[196:199], a[68:71], a[20:23], v[196:199]\n"
      "ds_read_b64_tr_b16 a[114:115], v211 offset:17152\n"
      "v_cvt_pk_bf16_f32 v191, v206, v207\n"
      "v_mfma_f32_16x16x32_bf16 v[196:199], a[64:67], a[16:19], v[196:199]\n"
      "v_exp_f32 v192, v192\n"
      "v_mfma_f32_16x16x32_bf16 v[200:203], a[64:67], a[32:35], 0\n"
      "ds_read_b128 a[88:91], v209 offset:1280\n"
      "ds_read_b64_tr_b16 a[164:165], v220 offset:1024\n"
      "v_mfma_f32_16x16x32_bf16 v[200:203], a[68:71], a[36:39], v[200:203]\n"
      "v_exp_f32 v193, v193\n"
      "v_mfma_f32_16x16x32_bf16 v[200:203], a[72:75], a[40:43], v[200:203]\n"
      "ds_read_b64_tr_b16 a[116:117], v210 offset:25088\n"
      "v_cvt_pk_bf16_f32 v128, v192, v193\n"
      "v_mfma_f32_16x16x32_bf16 v[200:203], a[76:79], a[44:47], v[200:203]\n"
      "v_exp_f32 v194, v194\n"
      "v_mfma_f32_16x16x32_bf16 v[204:207], a[76:79], a[60:63], 0\n"
      "ds_read_b128 a[92:95], v209 offset:1792\n"
      "s_waitcnt lgkmcnt(10)\n"
      "v_mfma_f32_16x16x32_bf16 v[204:207], a[72:75], a[56:59], v[204:207]\n"
      "v_exp_f32 v195, v195\n"
      "v_mfma_f32_16x16x32_bf16 v[204:207], a[68:71], a[52:55], v[204:207]\n"
      "ds_read_b64_tr_b16 a[118:119], v211 offset:25344\n"
      "v_cvt_pk_bf16_f32 v129, v194, v195\n"
      "v_mfma_f32_16x16x32_bf16 v[204:207], a[64:67], a[48:51], v[204:207]\n"
      "v_exp_f32 v196, v196\n"
      "v_mfma_f32_16x16x32_bf16 v[0:3], a[96:99], v[160:163], v[0:3]\n"
      "ds_read_b64_tr_b16 a[120:121], v212 offset:16896\n"
      "ds_read_b64_tr_b16 a[166:167], v221 offset:1280\n"
      "v_mfma_f32_16x16x32_bf16 v[0:3], a[100:103], v[164:167], v[0:3]\n"
      "v_exp_f32 v197, v197\n"
      "v_mfma_f32_16x16x32_bf16 v[32:35], a[100:103], v[172:175], v[32:35]\n"
      "ds_read_b64_tr_b16 a[122:123], v213 offset:17152\n"
      "v_cvt_pk_bf16_f32 v136, v196, v197\n"
      "v_mfma_f32_16x16x32_bf16 v[32:35], a[96:99], v[168:171], v[32:35]\n"
      "v_exp_f32 v198, v198\n"
      "v_mfma_f32_16x16x32_bf16 v[64:67], a[96:99], v[176:179], v[64:67]\n"
      "ds_read_b64_tr_b16 a[124:125], v212 offset:25088\n"
      "ds_read_b64_tr_b16 a[168:169], v218 offset:1536\n"
      "v_mfma_f32_16x16x32_bf16 v[64:67], a[100:103], v[180:183], v[64:67]\n"
      "v_exp_f32 v199, v199\n"
      "v_mfma_f32_16x16x32_bf16 v[96:99], a[100:103], v[188:191], v[96:99]\n"
      "ds_read_b64_tr_b16 a[126:127], v213 offset:25344\n"
      "v_cvt_pk_bf16_f32 v137, v198, v199\n"
      "v_mfma_f32_16x16x32_bf16 v[96:99], a[96:99], v[184:187], v[96:99]\n"
      "v_exp_f32 v200, v200\n"
      "v_mfma_f32_16x16x32_bf16 v[100:103], a[104:107], v[184:187], v[100:103]\n"
      "buffer_load_dwordx4 v216, s[40:43], s80 offen offset:2048 lds\n"
      "ds_read_b64_tr_b16 a[170:171], v219 offset:1792\n"
      "v_mfma_f32_16x16x32_bf16 v[100:103], a[108:111], v[188:191], v[100:103]\n"
      "v_exp_f32 v201, v201\n"
      "v_mfma_f32_16x16x32_bf16 v[68:71], a[108:111], v[180:183], v[68:71]\n"
      "ds_read_b64_tr_b16 a[172:173], v220 offset:1536\n"
      "v_cvt_pk_bf16_f32 v144, v200, v201\n"
      "v_mfma_f32_16x16x32_bf16 v[68:71], a[104:107], v[176:179], v[68:71]\n"
      "v_exp_f32 v202, v202\n"
      "v_mfma_f32_16x16x32_bf16 v[36:39], a[104:107], v[168:171], v[36:39]\n"
      "buffer_load_dwordx4 v217, s[40:43], s80 offen offset:3072 lds\n"
      "ds_read_b64_tr_b16 a[174:175], v221 offset:1792\n"
      "v_mfma_f32_16x16x32_bf16 v[36:39], a[108:111], v[172:175], v[36:39]\n"
      "v_exp_f32 v203, v203\n"
      "v_mfma_f32_16x16x32_bf16 v[4:7], a[108:111], v[164:167], v[4:7]\n"
      "v_cvt_pk_bf16_f32 v145, v202, v203\n"
      "s_waitcnt lgkmcnt(10)\n"
      "v_mfma_f32_16x16x32_bf16 v[4:7], a[104:107], v[160:163], v[4:7]\n"
      "v_exp_f32 v204, v204\n"
      "v_mfma_f32_16x16x32_bf16 v[192:195], a[80:83], a[0:3], 0\n"
      "ds_read_b128 a[64:67], v208 offset:8192\n"
      "v_and_b32 v236, v234, v236\n"
      "v_mfma_f32_16x16x32_bf16 v[192:195], a[84:87], a[4:7], v[192:195]\n"
      "v_exp_f32 v205, v205\n"
      "v_mfma_f32_16x16x32_bf16 v[192:195], a[88:91], a[8:11], v[192:195]\n"
      "ds_read_b64_tr_b16 a[96:97], v210 offset:17408\n"
      "v_cvt_pk_bf16_f32 v152, v204, v205\n"
      "v_mfma_f32_16x16x32_bf16 v[192:195], a[92:95], a[12:15], v[192:195]\n"
      "v_exp_f32 v206, v206\n"
      "v_mfma_f32_16x16x32_bf16 v[196:199], a[92:95], a[28:31], 0\n"
      "ds_read_b128 a[68:71], v208 offset:8704\n"
      "v_and_b32 v237, v234, v237\n"
      "v_mfma_f32_16x16x32_bf16 v[196:199], a[88:91], a[24:27], v[196:199]\n"
      "v_exp_f32 v207, v207\n"
      "v_mfma_f32_16x16x32_bf16 v[196:199], a[84:87], a[20:23], v[196:199]\n"
      "ds_read_b64_tr_b16 a[98:99], v211 offset:17664\n"
      "v_cvt_pk_bf16_f32 v153, v206, v207\n"
      "v_mfma_f32_16x16x32_bf16 v[196:199], a[80:83], a[16:19], v[196:199]\n"
      "v_exp_f32 v192, v192\n"
      "v_mfma_f32_16x16x32_bf16 v[200:203], a[80:83], a[32:35], 0\n"
      "ds_read_b128 a[72:75], v208 offset:9216\n"
      "v_and_b32 v238, v234, v238\n"
      "v_mfma_f32_16x16x32_bf16 v[200:203], a[84:87], a[36:39], v[200:203]\n"
      "v_exp_f32 v193, v193\n"
      "v_mfma_f32_16x16x32_bf16 v[200:203], a[88:91], a[40:43], v[200:203]\n"
      "ds_read_b64_tr_b16 a[100:101], v210 offset:25600\n"
      "v_cvt_pk_bf16_f32 v130, v192, v193\n"
      "v_mfma_f32_16x16x32_bf16 v[200:203], a[92:95], a[44:47], v[200:203]\n"
      "v_exp_f32 v194, v194\n"
      "v_mfma_f32_16x16x32_bf16 v[204:207], a[92:95], a[60:63], 0\n"
      "ds_read_b128 a[76:79], v208 offset:9728\n"
      "s_waitcnt lgkmcnt(7)\n"
      "v_mfma_f32_16x16x32_bf16 v[204:207], a[88:91], a[56:59], v[204:207]\n"
      "v_exp_f32 v195, v195\n"
      "v_mfma_f32_16x16x32_bf16 v[204:207], a[84:87], a[52:55], v[204:207]\n"
      "ds_read_b64_tr_b16 a[102:103], v211 offset:25856\n"
      "v_cvt_pk_bf16_f32 v131, v194, v195\n"
      "v_mfma_f32_16x16x32_bf16 v[204:207], a[80:83], a[48:51], v[204:207]\n"
      "v_exp_f32 v196, v196\n"
      "v_mfma_f32_16x16x32_bf16 v[8:11], a[112:115], v[160:163], v[8:11]\n"
      "ds_read_b64_tr_b16 a[104:105], v212 offset:17408\n"
      "v_and_b32 v239, v234, v239\n"
      "v_mfma_f32_16x16x32_bf16 v[8:11], a[116:119], v[164:167], v[8:11]\n"
      "v_exp_f32 v197, v197\n"
      "v_mfma_f32_16x16x32_bf16 v[40:43], a[116:119], v[172:175], v[40:43]\n"
      "ds_read_b64_tr_b16 a[106:107], v213 offset:17664\n"
      "v_cvt_pk_bf16_f32 v138, v196, v197\n"
      "v_mfma_f32_16x16x32_bf16 v[40:43], a[112:115], v[168:171], v[40:43]\n"
      "v_exp_f32 v198, v198\n"
      "v_mfma_f32_16x16x32_bf16 v[72:75], a[112:115], v[176:179], v[72:75]\n"
      "ds_read_b64_tr_b16 a[108:109], v212 offset:25600\n"
      "s_add_u32 m0, s90, 896\n"
      "v_mfma_f32_16x16x32_bf16 v[72:75], a[116:119], v[180:183], v[72:75]\n"
      "v_exp_f32 v199, v199\n"
      "v_mfma_f32_16x16x32_bf16 v[104:107], a[116:119], v[188:191], v[104:107]\n"
      "ds_read_b64_tr_b16 a[110:111], v213 offset:25856\n"
      "v_cvt_pk_bf16_f32 v139, v198, v199\n"
      "v_mfma_f32_16x16x32_bf16 v[104:107], a[112:115], v[184:187], v[104:107]\n"
      "v_exp_f32 v200, v200\n"
      "v_mfma_f32_16x16x32_bf16 a[144:147], a[160:163], v[228:231], a[144:147]\n"
      "buffer_load_dwordx4 v224, s[40:43], 0 offen offset:128 lds\n"
      "v_mfma_f32_16x16x32_bf16 v[108:111], a[120:123], v[184:187], v[108:111]\n"
      "s_add_u32 s80, s80, 0x4000\n"
      "v_mfma_f32_16x16x32_bf16 v[108:111], a[124:127], v[188:191], v[108:111]\n"
      "v_exp_f32 v201, v201\n"
      "v_mfma_f32_16x16x32_bf16 v[76:79], a[124:127], v[180:183], v[76:79]\n"
      "v_cvt_pk_bf16_f32 v146, v200, v201\n"
      "v_mfma_f32_16x16x32_bf16 v[76:79], a[120:123], v[176:179], v[76:79]\n"
      "v_exp_f32 v202, v202\n"
      "v_mfma_f32_16x16x32_bf16 a[148:151], a[164:167], v[228:231], a[148:151]\n"
      "buffer_load_dwordx4 v225, s[40:43], 0 offen offset:2176 lds\n"
      "v_mfma_f32_16x16x32_bf16 v[44:47], a[120:123], v[168:171], v[44:47]\n"
      "v_mfma_f32_16x16x32_bf16 v[44:47], a[124:127], v[172:175], v[44:47]\n"
      "v_exp_f32 v203, v203\n"
      "v_mfma_f32_16x16x32_bf16 v[12:15], a[124:127], v[164:167], v[12:15]\n"
      "v_cvt_pk_bf16_f32 v147, v202, v203\n"
      "s_waitcnt lgkmcnt(5)\n"
      "v_mfma_f32_16x16x32_bf16 v[12:15], a[120:123], v[160:163], v[12:15]\n"
      "v_exp_f32 v204, v204\n"
      "v_mfma_f32_16x16x32_bf16 v[192:195], a[64:67], a[0:3], 0\n"
      "ds_read_b128 a[80:83], v209 offset:8448\n"
      "v_mfma_f32_16x16x32_bf16 v[192:195], a[68:71], a[4:7], v[192:195]\n"
      "v_exp_f32 v205, v205\n"
      "v_mfma_f32_16x16x32_bf16 v[192:195], a[72:75], a[8:11], v[192:195]\n"
      "ds_read_b64_tr_b16 a[112:113], v210 offset:17920\n"
      "v_cvt_pk_bf16_f32 v154, v204, v205\n"
      "v_mfma_f32_16x16x32_bf16 v[192:195], a[76:79], a[12:15], v[192:195]\n"
      "v_exp_f32 v206, v206\n"
      "v_mfma_f32_16x16x32_bf16 v[196:199], a[76:79], a[28:31], 0\n"
      "ds_read_b128 a[84:87], v209 offset:8960\n"
      "v_mfma_f32_16x16x32_bf16 v[196:199], a[72:75], a[24:27], v[196:199]\n"
      "v_exp_f32 v207, v207\n"
      "v_mfma_f32_16x16x32_bf16 v[196:199], a[68:71], a[20:23], v[196:199]\n"
      "ds_read_b64_tr_b16 a[114:115], v211 offset:18176\n"
      "v_cvt_pk_bf16_f32 v155, v206, v207\n"
      "v_mfma_f32_16x16x32_bf16 v[196:199], a[64:67], a[16:19], v[196:199]\n"
      "v_exp_f32 v192, v192\n"
      "v_mfma_f32_16x16x32_bf16 v[200:203], a[64:67], a[32:35], 0\n"
      "ds_read_b128 a[88:91], v209 offset:9472\n"
      "v_mfma_f32_16x16x32_bf16 v[200:203], a[68:71], a[36:39], v[200:203]\n"
      "v_exp_f32 v193, v193\n"
      "v_mfma_f32_16x16x32_bf16 v[200:203], a[72:75], a[40:43], v[200:203]\n"
      "ds_read_b64_tr_b16 a[116:117], v210 offset:26112\n"
      "v_cvt_pk_bf16_f32 v132, v192, v193\n"
      "v_mfma_f32_16x16x32_bf16 v[200:203], a[76:79], a[44:47], v[200:203]\n"
      "v_exp_f32 v194, v194\n"
      "v_mfma_f32_16x16x32_bf16 v[204:207], a[76:79], a[60:63], 0\n"
      "ds_read_b128 a[92:95], v209 offset:9984\n"
      "s_waitcnt lgkmcnt(7)\n"
      "v_mfma_f32_16x16x32_bf16 v[204:207], a[72:75], a[56:59], v[204:207]\n"
      "v_exp_f32 v195, v195\n"
      "v_mfma_f32_16x16x32_bf16 v[204:207], a[68:71], a[52:55], v[204:207]\n"
      "ds_read_b64_tr_b16 a[118:119], v211 offset:26368\n"
      "v_cvt_pk_bf16_f32 v133, v194, v195\n"
      "v_mfma_f32_16x16x32_bf16 v[204:207], a[64:67], a[48:51], v[204:207]\n"
      "v_exp_f32 v196, v196\n"
      "v_mfma_f32_16x16x32_bf16 v[16:19], a[96:99], v[160:163], v[16:19]\n"
      "ds_read_b64_tr_b16 a[120:121], v212 offset:17920\n"
      "v_mfma_f32_16x16x32_bf16 v[16:19], a[100:103], v[164:167], v[16:19]\n"
      "v_exp_f32 v197, v197\n"
      "v_mfma_f32_16x16x32_bf16 v[48:51], a[100:103], v[172:175], v[48:51]\n"
      "ds_read_b64_tr_b16 a[122:123], v213 offset:18176\n"
      "v_cvt_pk_bf16_f32 v140, v196, v197\n"
      "v_mfma_f32_16x16x32_bf16 v[48:51], a[96:99], v[168:171], v[48:51]\n"
      "v_exp_f32 v198, v198\n"
      "v_mfma_f32_16x16x32_bf16 v[80:83], a[96:99], v[176:179], v[80:83]\n"
      "ds_read_b64_tr_b16 a[124:125], v212 offset:26112\n"
      "s_add_u32 m0, s90, 4992\n"
      "v_mfma_f32_16x16x32_bf16 v[80:83], a[100:103], v[180:183], v[80:83]\n"
      "v_exp_f32 v199, v199\n"
      "v_mfma_f32_16x16x32_bf16 v[112:115], a[100:103], v[188:191], v[112:115]\n"
      "ds_read_b64_tr_b16 a[126:127], v213 offset:26368\n"
      "v_cvt_pk_bf16_f32 v141, v198, v199\n"
      "v_mfma_f32_16x16x32_bf16 v[112:115], a[96:99], v[184:187], v[112:115]\n"
      "v_exp_f32 v200, v200\n"
      "v_mfma_f32_16x16x32_bf16 a[152:155], a[168:171], v[228:231], a[152:155]\n"
      "buffer_load_dwordx4 v226, s[40:43], 0 offen offset:128 lds\n"
      "v_mfma_f32_16x16x32_bf16 v[116:119], a[104:107], v[184:187], v[116:119]\n"
      "v_add_u32 v210, s81, v241\n"
      "v_xor_b32 v211, 16, v210\n"
      "v_mfma_f32_16x16x32_bf16 v[116:119], a[108:111], v[188:191], v[116:119]\n"
      "v_exp_f32 v201, v201\n"
      "v_mfma_f32_16x16x32_bf16 v[84:87], a[108:111], v[180:183], v[84:87]\n"
      "v_cvt_pk_bf16_f32 v148, v200, v201\n"
      "v_xor_b32 v212, 32, v210\n"
      "v_mfma_f32_16x16x32_bf16 v[84:87], a[104:107], v[176:179], v[84:87]\n"
      "v_exp_f32 v202, v202\n"
      "v_mfma_f32_16x16x32_bf16 a[156:159], a[172:175], v[228:231], a[156:159]\n"
      "buffer_load_dwordx4 v227, s[40:43], 0 offen offset:2176 lds\n"
      "v_mfma_f32_16x16x32_bf16 v[52:55], a[104:107], v[168:171], v[52:55]\n"
      "ds_read2_b32 v[224:225], v222 offset0:0 offset1:8\n"
      "ds_read2_b32 v[226:227], v222 offset0:16 offset1:24\n"
      "v_mfma_f32_16x16x32_bf16 v[52:55], a[108:111], v[172:175], v[52:55]\n"
      "v_exp_f32 v203, v203\n"
      "v_mfma_f32_16x16x32_bf16 v[20:23], a[108:111], v[164:167], v[20:23]\n"
      "v_cvt_pk_bf16_f32 v149, v202, v203\n"
      "s_waitcnt vmcnt(12) lgkmcnt(2)\n"
      "s_barrier\n"
      "v_mfma_f32_16x16x32_bf16 v[20:23], a[104:107], v[160:163], v[20:23]\n"
      "v_exp_f32 v204, v204\n"
      "v_mfma_f32_16x16x32_bf16 v[192:195], a[80:83], a[0:3], 0\n"
      "ds_read_b128 a[64:67], v208 offset:16384\n"
      "v_xor_b32 v213, 48, v210\n"
      "v_mfma_f32_16x16x32_bf16 v[192:195], a[84:87], a[4:7], v[192:195]\n"
      "v_exp_f32 v205, v205\n"
      "v_mfma_f32_16x16x32_bf16 v[192:195], a[88:91], a[8:11], v[192:195]\n"
      "ds_read_b64_tr_b16 a[96:97], v210 offset:0\n"
      "v_cvt_pk_bf16_f32 v156, v204, v205\n"
      "v_mfma_f32_16x16x32_bf16 v[192:195], a[92:95], a[12:15], v[192:195]\n"
      "v_exp_f32 v206, v206\n"
      "v_mfma_f32_16x16x32_bf16 v[196:199], a[92:95], a[28:31], 0\n"
      "ds_read_b128 a[68:71], v208 offset:16896\n"
      "v_lshlrev_b32 v228, v235, v236\n"
      "v_mfma_f32_16x16x32_bf16 v[196:199], a[88:91], a[24:27], v[196:199]\n"
      "v_exp_f32 v207, v207\n"
      "v_mfma_f32_16x16x32_bf16 v[196:199], a[84:87], a[20:23], v[196:199]\n"
      "ds_read_b64_tr_b16 a[98:99], v211 offset:256\n"
      "v_cvt_pk_bf16_f32 v157, v206, v207\n"
      "v_mfma_f32_16x16x32_bf16 v[196:199], a[80:83], a[16:19], v[196:199]\n"
      "v_exp_f32 v192, v192\n"
      "v_mfma_f32_16x16x32_bf16 v[200:203], a[80:83], a[32:35], 0\n"
      "ds_read_b128 a[72:75], v208 offset:17408\n"
      "v_lshlrev_b32 v229, v235, v237\n"
      "v_mfma_f32_16x16x32_bf16 v[200:203], a[84:87], a[36:39], v[200:203]\n"
      "v_exp_f32 v193, v193\n"
      "v_mfma_f32_16x16x32_bf16 v[200:203], a[88:91], a[40:43], v[200:203]\n"
      "ds_read_b64_tr_b16 a[100:101], v210 offset:8192\n"
      "v_cvt_pk_bf16_f32 v134, v192, v193\n"
      "v_mfma_f32_16x16x32_bf16 v[200:203], a[92:95], a[44:47], v[200:203]\n"
      "v_exp_f32 v194, v194\n"
      "v_mfma_f32_16x16x32_bf16 v[204:207], a[92:95], a[60:63], 0\n"
      "ds_read_b128 a[76:79], v208 offset:17920\n"
      "v_lshlrev_b32 v230, v235, v238\n"
      "v_mfma_f32_16x16x32_bf16 v[204:207], a[88:91], a[56:59], v[204:207]\n"
      "v_exp_f32 v195, v195\n"
      "v_mfma_f32_16x16x32_bf16 v[204:207], a[84:87], a[52:55], v[204:207]\n"
      "ds_read_b64_tr_b16 a[102:103], v211 offset:8448\n"
      "v_cvt_pk_bf16_f32 v135, v194, v195\n"
      "v_mfma_f32_16x16x32_bf16 v[204:207], a[80:83], a[48:51], v[204:207]\n"
      "v_exp_f32 v196, v196\n"
      "v_mfma_f32_16x16x32_bf16 v[24:27], a[112:115], v[160:163], v[24:27]\n"
      "ds_read_b64_tr_b16 a[104:105], v212 offset:0\n"
      "v_lshlrev_b32 v231, v235, v239\n"
      "v_mfma_f32_16x16x32_bf16 v[24:27], a[116:119], v[164:167], v[24:27]\n"
      "v_exp_f32 v197, v197\n"
      "v_mfma_f32_16x16x32_bf16 v[56:59], a[116:119], v[172:175], v[56:59]\n"
      "ds_read_b64_tr_b16 a[106:107], v213 offset:256\n"
      "v_cvt_pk_bf16_f32 v142, v196, v197\n"
      "v_mfma_f32_16x16x32_bf16 v[56:59], a[112:115], v[168:171], v[56:59]\n"
      "v_exp_f32 v198, v198\n"
      "v_mfma_f32_16x16x32_bf16 v[88:91], a[112:115], v[176:179], v[88:91]\n"
      "ds_read_b64_tr_b16 a[108:109], v212 offset:8192\n"
      "s_mov_b32 m0, s87\n"
      "v_mfma_f32_16x16x32_bf16 v[88:91], a[116:119], v[180:183], v[88:91]\n"
      "v_exp_f32 v199, v199\n"
      "v_mfma_f32_16x16x32_bf16 v[120:123], a[116:119], v[188:191], v[120:123]\n"
      "ds_read_b64_tr_b16 a[110:111], v213 offset:8448\n"
      "v_cvt_pk_bf16_f32 v143, v198, v199\n"
      "v_mfma_f32_16x16x32_bf16 v[120:123], a[112:115], v[184:187], v[120:123]\n"
      "v_exp_f32 v200, v200\n"
      "v_mfma_f32_16x16x32_bf16 v[124:127], a[120:123], v[184:187], v[124:127]\n"
      "buffer_load_dwordx4 v214, s[40:43], s80 offen offset:0 lds\n"
      "s_mov_b32 s87, s81\n"
      "v_mfma_f32_16x16x32_bf16 v[124:127], a[124:127], v[188:191], v[124:127]\n"
      "v_exp_f32 v201, v201\n"
      "v_mfma_f32_16x16x32_bf16 v[92:95], a[124:127], v[180:183], v[92:95]\n"
      "v_cvt_pk_bf16_f32 v150, v200, v201\n"
      "s_mov_b32 s81, s86\n"
      "v_mfma_f32_16x16x32_bf16 v[92:95], a[120:123], v[176:179], v[92:95]\n"
      "v_exp_f32 v202, v202\n"
      "v_mfma_f32_16x16x32_bf16 v[60:63], a[120:123], v[168:171], v[60:63]\n"
      "buffer_load_dwordx4 v215, s[40:43], s80 offen offset:1024 lds\n"
      "s_mov_b32 s86, m0\n"
      "v_mfma_f32_16x16x32_bf16 v[60:63], a[124:127], v[172:175], v[60:63]\n"
      "v_exp_f32 v203, v203\n"
      "v_mfma_f32_16x16x32_bf16 v[28:31], a[124:127], v[164:167], v[28:31]\n"
      "v_cvt_pk_bf16_f32 v151, v202, v203\n"
      "s_waitcnt vmcnt(10) lgkmcnt(5)\n"
      "v_mfma_f32_16x16x32_bf16 v[28:31], a[120:123], v[160:163], v[28:31]\n"
      "v_exp_f32 v204, v204\n"
      "s_sub_u32 s88, s88, 1\n"
      "s_cbranch_scc0 1b\n"
      ".p2align 5\n"
      "2:\n"
      "v_mfma_f32_16x16x32_bf16 v[192:195], a[64:67], a[0:3], 0\n"
      "ds_read_b128 a[80:83], v209 offset:16640\n"
      "v_mfma_f32_16x16x32_bf16 v[192:195], a[68:71], a[4:7], v[192:195]\n"
      "v_exp_f32 v205, v205\n"
      "v_mfma_f32_16x16x32_bf16 v[192:195], a[72:75], a[8:11], v[192:195]\n"
      "ds_read_b64_tr_b16 a[112:113], v210 offset:512\n"
      "v_cvt_pk_bf16_f32 v158, v204, v205\n"
      "v_mfma_f32_16x16x32_bf16 v[192:195], a[76:79], a[12:15], v[192:195]\n"
      "v_exp_f32 v206, v206\n"
      "v_mfma_f32_16x16x32_bf16 v[196:199], a[76:79], a[28:31], 0\n"
      "ds_read_b128 a[84:87], v209 offset:17152\n"
      "v_mfma_f32_16x16x32_bf16 v[196:199], a[72:75], a[24:27], v[196:199]\n"
      "v_exp_f32 v207, v207\n"
      "v_mfma_f32_16x16x32_bf16 v[196:199], a[68:71], a[20:23], v[196:199]\n"
      "ds_read_b64_tr_b16 a[114:115], v211 offset:768\n"
      "v_cvt_pk_bf16_f32 v159, v206, v207\n"
      "v_mfma_f32_16x16x32_bf16 v[196:199], a[64:67], a[16:19], v[196:199]\n"
      "v_exp_f32 v192, v192\n"
      "v_mfma_f32_16x16x32_bf16 v[200:203], a[64:67], a[32:35], 0\n"
      "ds_read_b128 a[88:91], v209 offset:17664\n"
      "v_mfma_f32_16x16x32_bf16 v[200:203], a[68:71], a[36:39], v[200:203]\n"
      "v_exp_f32 v193, v193\n"
      "v_mfma_f32_16x16x32_bf16 v[200:203], a[72:75], a[40:43], v[200:203]\n"
      "ds_read_b64_tr_b16 a[116:117], v210 offset:8704\n"
      "v_cvt_pk_bf16_f32 v160, v192, v193\n"
      "v_mfma_f32_16x16x32_bf16 v[200:203], a[76:79], a[44:47], v[200:203]\n"
      "v_exp_f32 v194, v194\n"
      "v_mfma_f32_16x16x32_bf16 v[204:207], a[76:79], a[60:63], 0\n"
      "ds_read_b128 a[92:95], v209 offset:18176\n"
      "s_waitcnt lgkmcnt(7)\n"
      "v_mfma_f32_16x16x32_bf16 v[204:207], a[72:75], a[56:59], v[204:207]\n"
      "v_exp_f32 v195, v195\n"
      "v_mfma_f32_16x16x32_bf16 v[204:207], a[68:71], a[52:55], v[204:207]\n"
      "ds_read_b64_tr_b16 a[118:119], v211 offset:8960\n"
      "v_cvt_pk_bf16_f32 v161, v194, v195\n"
      "v_mfma_f32_16x16x32_bf16 v[204:207], a[64:67], a[48:51], v[204:207]\n"
      "v_exp_f32 v196, v196\n"
      "v_mfma_f32_16x16x32_bf16 v[0:3], a[96:99], v[128:131], v[0:3]\n"
      "ds_read_b64_tr_b16 a[120:121], v212 offset:512\n"
      "v_mfma_f32_16x16x32_bf16 v[0:3], a[100:103], v[132:135], v[0:3]\n"
      "v_exp_f32 v197, v197\n"
      "v_mfma_f32_16x16x32_bf16 v[32:35], a[100:103], v[140:143], v[32:35]\n"
      "ds_read_b64_tr_b16 a[122:123], v213 offset:768\n"
      "v_cvt_pk_bf16_f32 v168, v196, v197\n"
      "v_mfma_f32_16x16x32_bf16 v[32:35], a[96:99], v[136:139], v[32:35]\n"
      "v_exp_f32 v198, v198\n"
      "v_mfma_f32_16x16x32_bf16 v[64:67], a[96:99], v[144:147], v[64:67]\n"
      "ds_read_b64_tr_b16 a[124:125], v212 offset:8704\n"
      "v_mfma_f32_16x16x32_bf16 v[64:67], a[100:103], v[148:151], v[64:67]\n"
      "v_exp_f32 v199, v199\n"
      "v_mfma_f32_16x16x32_bf16 v[96:99], a[100:103], v[156:159], v[96:99]\n"
      "ds_read_b64_tr_b16 a[126:127], v213 offset:8960\n"
      "v_cvt_pk_bf16_f32 v169, v198, v199\n"
      "v_mfma_f32_16x16x32_bf16 v[96:99], a[96:99], v[152:155], v[96:99]\n"
      "v_exp_f32 v200, v200\n"
      "v_mfma_f32_16x16x32_bf16 v[100:103], a[104:107], v[152:155], v[100:103]\n"
      "buffer_load_dwordx4 v216, s[40:43], s80 offen offset:2048 lds\n"
      "v_mfma_f32_16x16x32_bf16 v[100:103], a[108:111], v[156:159], v[100:103]\n"
      "v_exp_f32 v201, v201\n"
      "v_mfma_f32_16x16x32_bf16 v[68:71], a[108:111], v[148:151], v[68:71]\n"
      "v_cvt_pk_bf16_f32 v176, v200, v201\n"
      "v_mfma_f32_16x16x32_bf16 v[68:71], a[104:107], v[144:147], v[68:71]\n"
      "v_exp_f32 v202, v202\n"
      "v_mfma_f32_16x16x32_bf16 v[36:39], a[104:107], v[136:139], v[36:39]\n"
      "buffer_load_dwordx4 v217, s[40:43], s80 offen offset:3072 lds\n"
      "v_mfma_f32_16x16x32_bf16 v[36:39], a[108:111], v[140:143], v[36:39]\n"
      "v_exp_f32 v203, v203\n"
      "v_mfma_f32_16x16x32_bf16 v[4:7], a[108:111], v[132:135], v[4:7]\n"
      "v_cvt_pk_bf16_f32 v177, v202, v203\n"
      "s_waitcnt lgkmcnt(5)\n"
      "v_mfma_f32_16x16x32_bf16 v[4:7], a[104:107], v[128:131], v[4:7]\n"
      "v_exp_f32 v204, v204\n"
      "v_mfma_f32_16x16x32_bf16 v[192:195], a[80:83], a[0:3], 0\n"
      "ds_read_b128 a[64:67], v208 offset:24576\n"
      "v_mfma_f32_16x16x32_bf16 v[192:195], a[84:87], a[4:7], v[192:195]\n"
      "v_exp_f32 v205, v205\n"
      "v_mfma_f32_16x16x32_bf16 v[192:195], a[88:91], a[8:11], v[192:195]\n"
      "ds_read_b64_tr_b16 a[96:97], v210 offset:1024\n"
      "v_cvt_pk_bf16_f32 v184, v204, v205\n"
      "v_mfma_f32_16x16x32_bf16 v[192:195], a[92:95], a[12:15], v[192:195]\n"
      "v_exp_f32 v206, v206\n"
      "v_mfma_f32_16x16x32_bf16 v[196:199], a[92:95], a[28:31], 0\n"
      "ds_read_b128 a[68:71], v208 offset:25088\n"
      "v_mfma_f32_16x16x32_bf16 v[196:199], a[88:91], a[24:27], v[196:199]\n"
      "v_exp_f32 v207, v207\n"
      "v_mfma_f32_16x16x32_bf16 v[196:199], a[84:87], a[20:23], v[196:199]\n"
      "ds_read_b64_tr_b16 a[98:99], v211 offset:1280\n"
      "v_cvt_pk_bf16_f32 v185, v206, v207\n"
      "v_mfma_f32_16x16x32_bf16 v[196:199], a[80:83], a[16:19], v[196:199]\n"
      "v_exp_f32 v192, v192\n"
      "v_mfma_f32_16x16x32_bf16 v[200:203], a[80:83], a[32:35], 0\n"
      "ds_read_b128 a[72:75], v208 offset:25600\n"
      "v_mfma_f32_16x16x32_bf16 v[200:203], a[84:87], a[36:39], v[200:203]\n"
      "v_exp_f32 v193, v193\n"
      "v_mfma_f32_16x16x32_bf16 v[200:203], a[88:91], a[40:43], v[200:203]\n"
      "ds_read_b64_tr_b16 a[100:101], v210 offset:9216\n"
      "v_cvt_pk_bf16_f32 v162, v192, v193\n"
      "v_mfma_f32_16x16x32_bf16 v[200:203], a[92:95], a[44:47], v[200:203]\n"
      "v_exp_f32 v194, v194\n"
      "v_mfma_f32_16x16x32_bf16 v[204:207], a[92:95], a[60:63], 0\n"
      "ds_read_b128 a[76:79], v208 offset:26112\n"
      "s_waitcnt lgkmcnt(7)\n"
      "v_mfma_f32_16x16x32_bf16 v[204:207], a[88:91], a[56:59], v[204:207]\n"
      "v_exp_f32 v195, v195\n"
      "v_mfma_f32_16x16x32_bf16 v[204:207], a[84:87], a[52:55], v[204:207]\n"
      "ds_read_b64_tr_b16 a[102:103], v211 offset:9472\n"
      "v_cvt_pk_bf16_f32 v163, v194, v195\n"
      "v_mfma_f32_16x16x32_bf16 v[204:207], a[80:83], a[48:51], v[204:207]\n"
      "v_exp_f32 v196, v196\n"
      "v_mfma_f32_16x16x32_bf16 v[8:11], a[112:115], v[128:131], v[8:11]\n"
      "ds_read_b64_tr_b16 a[104:105], v212 offset:1024\n"
      "v_mfma_f32_16x16x32_bf16 v[8:11], a[116:119], v[132:135], v[8:11]\n"
      "v_exp_f32 v197, v197\n"
      "v_mfma_f32_16x16x32_bf16 v[40:43], a[116:119], v[140:143], v[40:43]\n"
      "ds_read_b64_tr_b16 a[106:107], v213 offset:1280\n"
      "v_cvt_pk_bf16_f32 v170, v196, v197\n"
      "v_mfma_f32_16x16x32_bf16 v[40:43], a[112:115], v[136:139], v[40:43]\n"
      "v_exp_f32 v198, v198\n"
      "v_mfma_f32_16x16x32_bf16 v[72:75], a[112:115], v[144:147], v[72:75]\n"
      "ds_read_b64_tr_b16 a[108:109], v212 offset:9216\n"
      "v_mfma_f32_16x16x32_bf16 v[72:75], a[116:119], v[148:151], v[72:75]\n"
      "v_exp_f32 v199, v199\n"
      "v_mfma_f32_16x16x32_bf16 v[104:107], a[116:119], v[156:159], v[104:107]\n"
      "ds_read_b64_tr_b16 a[110:111], v213 offset:9472\n"
      "v_cvt_pk_bf16_f32 v171, v198, v199\n"
      "v_mfma_f32_16x16x32_bf16 v[104:107], a[112:115], v[152:155], v[104:107]\n"
      "v_exp_f32 v200, v200\n"
      "v_mfma_f32_16x16x32_bf16 v[108:111], a[120:123], v[152:155], v[108:111]\n"
      "s_add_u32 s80, s80, 0x4000\n"
      "v_mfma_f32_16x16x32_bf16 v[108:111], a[124:127], v[156:159], v[108:111]\n"
      "v_exp_f32 v201, v201\n"
      "v_mfma_f32_16x16x32_bf16 v[76:79], a[124:127], v[148:151], v[76:79]\n"
      "v_cvt_pk_bf16_f32 v178, v200, v201\n"
      "s_cmp_eq_u32 s80, s89\n"
      "v_mfma_f32_16x16x32_bf16 v[76:79], a[120:123], v[144:147], v[76:79]\n"
      "v_exp_f32 v202, v202\n"
      "v_mfma_f32_16x16x32_bf16 v[44:47], a[120:123], v[136:139], v[44:47]\n"
      "s_cselect_b32 s80, s92, s80\n"
      "v_mfma_f32_16x16x32_bf16 v[44:47], a[124:127], v[140:143], v[44:47]\n"
      "v_exp_f32 v203, v203\n"
      "v_mfma_f32_16x16x32_bf16 v[12:15], a[124:127], v[132:135], v[12:15]\n"
      "v_cvt_pk_bf16_f32 v179, v202, v203\n"
      "s_waitcnt lgkmcnt(5)\n"
      "v_mfma_f32_16x16x32_bf16 v[12:15], a[120:123], v[128:131], v[12:15]\n"
      "v_exp_f32 v204, v204\n"
      "v_mfma_f32_16x16x32_bf16 v[192:195], a[64:67], a[0:3], 0\n"
      "ds_read_b128 a[80:83], v209 offset:24832\n"
      "v_lshlrev_b32 v224, 7, %[t]\n"
      "v_mfma_f32_16x16x32_bf16 v[192:195], a[68:71], a[4:7], v[192:195]\n"
      "v_exp_f32 v205, v205\n"
      "v_mfma_f32_16x16x32_bf16 v[192:195], a[72:75], a[8:11], v[192:195]\n"
      "ds_read_b64_tr_b16 a[112:113], v210 offset:1536\n"
      "v_cvt_pk_bf16_f32 v186, v204, v205\n"
      "v_mfma_f32_16x16x32_bf16 v[192:195], a[76:79], a[12:15], v[192:195]\n"
      "v_exp_f32 v206, v206\n"
      "v_mfma_f32_16x16x32_bf16 v[196:199], a[76:79], a[28:31], 0\n"
      "ds_read_b128 a[84:87], v209 offset:25344\n"
      "buffer_load_dword v225, v224, s[60:63], 0 offen\n"
      "v_mfma_f32_16x16x32_bf16 v[196:199], a[72:75], a[24:27], v[196:199]\n"
      "v_exp_f32 v207, v207\n"
      "v_mfma_f32_16x16x32_bf16 v[196:199], a[68:71], a[20:23], v[196:199]\n"
      "ds_read_b64_tr_b16 a[114:115], v211 offset:1792\n"
      "v_cvt_pk_bf16_f32 v187, v206, v207\n"
      "v_mfma_f32_16x16x32_bf16 v[196:199], a[64:67], a[16:19], v[196:199]\n"
      "v_exp_f32 v192, v192\n"
      "v_mfma_f32_16x16x32_bf16 v[200:203], a[64:67], a[32:35], 0\n"
      "ds_read_b128 a[88:91], v209 offset:25856\n"
      "v_mfma_f32_16x16x32_bf16 v[200:203], a[68:71], a[36:39], v[200:203]\n"
      "v_exp_f32 v193, v193\n"
      "v_mfma_f32_16x16x32_bf16 v[200:203], a[72:75], a[40:43], v[200:203]\n"
      "ds_read_b64_tr_b16 a[116:117], v210 offset:9728\n"
      "v_cvt_pk_bf16_f32 v164, v192, v193\n"
      "v_mfma_f32_16x16x32_bf16 v[200:203], a[76:79], a[44:47], v[200:203]\n"
      "v_exp_f32 v194, v194\n"
      "v_mfma_f32_16x16x32_bf16 v[204:207], a[76:79], a[60:63], 0\n"
      "ds_read_b128 a[92:95], v209 offset:26368\n"
      "s_waitcnt lgkmcnt(7)\n"
      "v_mfma_f32_16x16x32_bf16 v[204:207], a[72:75], a[56:59], v[204:207]\n"
      "v_exp_f32 v195, v195\n"
      "v_mfma_f32_16x16x32_bf16 v[204:207], a[68:71], a[52:55], v[204:207]\n"
      "ds_read_b64_tr_b16 a[118:119], v211 offset:9984\n"
      "v_cvt_pk_bf16_f32 v165, v194, v195\n"
      "v_mfma_f32_16x16x32_bf16 v[204:207], a[64:67], a[48:51], v[204:207]\n"
      "v_exp_f32 v196, v196\n"
      "v_mfma_f32_16x16x32_bf16 v[16:19], a[96:99], v[128:131], v[16:19]\n"
      "ds_read_b64_tr_b16 a[120:121], v212 offset:1536\n"
      "v_add_u32 v208, s81, v240\n"
      "v_mfma_f32_16x16x32_bf16 v[16:19], a[100:103], v[132:135], v[16:19]\n"
      "v_exp_f32 v197, v197\n"
      "v_mfma_f32_16x16x32_bf16 v[48:51], a[100:103], v[140:143], v[48:51]\n"
      "ds_read_b64_tr_b16 a[122:123], v213 offset:1792\n"
      "v_cvt_pk_bf16_f32 v172, v196, v197\n"
      "v_mfma_f32_16x16x32_bf16 v[48:51], a[96:99], v[136:139], v[48:51]\n"
      "v_exp_f32 v198, v198\n"
      "v_mfma_f32_16x16x32_bf16 v[80:83], a[96:99], v[144:147], v[80:83]\n"
      "ds_read_b64_tr_b16 a[124:125], v212 offset:9728\n"
      "v_mfma_f32_16x16x32_bf16 v[80:83], a[100:103], v[148:151], v[80:83]\n"
      "v_exp_f32 v199, v199\n"
      "v_mfma_f32_16x16x32_bf16 v[112:115], a[100:103], v[156:159], v[112:115]\n"
      "ds_read_b64_tr_b16 a[126:127], v213 offset:9984\n"
      "v_cvt_pk_bf16_f32 v173, v198, v199\n"
      "v_mfma_f32_16x16x32_bf16 v[112:115], a[96:99], v[152:155], v[112:115]\n"
      "v_exp_f32 v200, v200\n"
      "v_mfma_f32_16x16x32_bf16 v[116:119], a[104:107], v[152:155], v[116:119]\n"
      "v_mfma_f32_16x16x32_bf16 v[116:119], a[108:111], v[156:159], v[116:119]\n"
      "v_exp_f32 v201, v201\n"
      "v_mfma_f32_16x16x32_bf16 v[84:87], a[108:111], v[148:151], v[84:87]\n"
      "v_cvt_pk_bf16_f32 v180, v200, v201\n"
      "v_xor_b32 v209, 16, v208\n"
      "v_mfma_f32_16x16x32_bf16 v[84:87], a[104:107], v[144:147], v[84:87]\n"
      "v_exp_f32 v202, v202\n"
      "v_mfma_f32_16x16x32_bf16 v[52:55], a[104:107], v[136:139], v[52:55]\n"
      "v_mfma_f32_16x16x32_bf16 v[52:55], a[108:111], v[140:143], v[52:55]\n"
      "v_exp_f32 v203, v203\n"
      "v_mfma_f32_16x16x32_bf16 v[20:23], a[108:111], v[132:135], v[20:23]\n"
      "v_cvt_pk_bf16_f32 v181, v202, v203\n"
      "s_waitcnt lgkmcnt(5)\n"
      "v_mfma_f32_16x16x32_bf16 v[20:23], a[104:107], v[128:131], v[20:23]\n"
      "v_exp_f32 v204, v204\n"
      "v_mfma_f32_16x16x32_bf16 v[192:195], a[80:83], a[0:3], 0\n"
      "ds_read_b128 a[64:67], v208 offset:0\n"
      "v_mfma_f32_16x16x32_bf16 v[192:195], a[84:87], a[4:7], v[192:195]\n"
      "v_exp_f32 v205, v205\n"
      "v_mfma_f32_16x16x32_bf16 v[192:195], a[88:91], a[8:11], v[192:195]\n"
      "ds_read_b64_tr_b16 a[96:97], v210 offset:16384\n"
      "v_cvt_pk_bf16_f32 v188, v204, v205\n"
      "v_mfma_f32_16x16x32_bf16 v[192:195], a[92:95], a[12:15], v[192:195]\n"
      "v_exp_f32 v206, v206\n"
      "v_mfma_f32_16x16x32_bf16 v[196:199], a[92:95], a[28:31], 0\n"
      "ds_read_b128 a[68:71], v208 offset:512\n"
      "v_mfma_f32_16x16x32_bf16 v[196:199], a[88:91], a[24:27], v[196:199]\n"
      "v_exp_f32 v207, v207\n"
      "v_mfma_f32_16x16x32_bf16 v[196:199], a[84:87], a[20:23], v[196:199]\n"
      "ds_read_b64_tr_b16 a[98:99], v211 offset:16640\n"
      "v_cvt_pk_bf16_f32 v189, v206, v207\n"
      "v_mfma_f32_16x16x32_bf16 v[196:199], a[80:83], a[16:19], v[196:199]\n"
      "v_exp_f32 v192, v192\n"
      "v_mfma_f32_16x16x32_bf16 v[200:203], a[80:83], a[32:35], 0\n"
      "ds_read_b128 a[72:75], v208 offset:1024\n"
      "v_mfma_f32_16x16x32_bf16 v[200:203], a[84:87], a[36:39], v[200:203]\n"
      "v_exp_f32 v193, v193\n"
      "v_mfma_f32_16x16x32_bf16 v[200:203], a[88:91], a[40:43], v[200:203]\n"
      "ds_read_b64_tr_b16 a[100:101], v210 offset:24576\n"
      "v_cvt_pk_bf16_f32 v166, v192, v193\n"
      "v_mfma_f32_16x16x32_bf16 v[200:203], a[92:95], a[44:47], v[200:203]\n"
      "v_exp_f32 v194, v194\n"
      "v_mfma_f32_16x16x32_bf16 v[204:207], a[92:95], a[60:63], 0\n"
      "ds_read_b128 a[76:79], v208 offset:1536\n"
      "s_waitcnt lgkmcnt(7)\n"
      "v_mfma_f32_16x16x32_bf16 v[204:207], a[88:91], a[56:59], v[204:207]\n"
      "v_exp_f32 v195, v195\n"
      "v_mfma_f32_16x16x32_bf16 v[204:207], a[84:87], a[52:55], v[204:207]\n"
      "ds_read_b64_tr_b16 a[102:103], v211 offset:24832\n"
      "v_cvt_pk_bf16_f32 v167, v194, v195\n"
      "v_mfma_f32_16x16x32_bf16 v[204:207], a[80:83], a[48:51], v[204:207]\n"
      "v_exp_f32 v196, v196\n"
      "v_mfma_f32_16x16x32_bf16 v[24:27], a[112:115], v[128:131], v[24:27]\n"
      "ds_read_b64_tr_b16 a[104:105], v212 offset:16384\n"
      "v_mfma_f32_16x16x32_bf16 v[24:27], a[116:119], v[132:135], v[24:27]\n"
      "v_exp_f32 v197, v197\n"
      "v_mfma_f32_16x16x32_bf16 v[56:59], a[116:119], v[140:143], v[56:59]\n"
      "ds_read_b64_tr_b16 a[106:107], v213 offset:16640\n"
      "v_cvt_pk_bf16_f32 v174, v196, v197\n"
      "v_mfma_f32_16x16x32_bf16 v[56:59], a[112:115], v[136:139], v[56:59]\n"
      "v_exp_f32 v198, v198\n"
      "v_mfma_f32_16x16x32_bf16 v[88:91], a[112:115], v[144:147], v[88:91]\n"
      "ds_read_b64_tr_b16 a[108:109], v212 offset:24576\n"
      "s_add_u32 m0, s86, 0x4000\n"
      "v_mfma_f32_16x16x32_bf16 v[88:91], a[116:119], v[148:151], v[88:91]\n"
      "v_exp_f32 v199, v199\n"
      "v_mfma_f32_16x16x32_bf16 v[120:123], a[116:119], v[156:159], v[120:123]\n"
      "ds_read_b64_tr_b16 a[110:111], v213 offset:24832\n"
      "v_cvt_pk_bf16_f32 v175, v198, v199\n"
      "v_mfma_f32_16x16x32_bf16 v[120:123], a[112:115], v[152:155], v[120:123]\n"
      "v_exp_f32 v200, v200\n"
      "v_mfma_f32_16x16x32_bf16 v[124:127], a[120:123], v[152:155], v[124:127]\n"
      "buffer_load_dwordx4 v214, s[40:43], s80 offen offset:0 lds\n"
      "v_mfma_f32_16x16x32_bf16 v[124:127], a[124:127], v[156:159], v[124:127]\n"
      "v_exp_f32 v201, v201\n"
      "v_mfma_f32_16x16x32_bf16 v[92:95], a[124:127], v[148:151], v[92:95]\n"
      "v_cvt_pk_bf16_f32 v182, v200, v201\n"
      "v_mfma_f32_16x16x32_bf16 v[92:95], a[120:123], v[144:147], v[92:95]\n"
      "v_exp_f32 v202, v202\n"
      "v_mfma_f32_16x16x32_bf16 v[60:63], a[120:123], v[136:139], v[60:63]\n"
      "buffer_load_dwordx4 v215, s[40:43], s80 offen offset:1024 lds\n"
      "v_mfma_f32_16x16x32_bf16 v[60:63], a[124:127], v[140:143], v[60:63]\n"
      "v_exp_f32 v203, v203\n"
      "v_mfma_f32_16x16x32_bf16 v[28:31], a[124:127], v[132:135], v[28:31]\n"
      "v_cvt_pk_bf16_f32 v183, v202, v203\n"
      "s_waitcnt lgkmcnt(5)\n"
      "v_mfma_f32_16x16x32_bf16 v[28:31], a[120:123], v[128:131], v[28:31]\n"
      "v_exp_f32 v204, v204\n"
      "v_mfma_f32_16x16x32_bf16 v[192:195], a[64:67], a[0:3], 0\n"
      "ds_read_b128 a[80:83], v209 offset:256\n"
      "v_mfma_f32_16x16x32_bf16 v[192:195], a[68:71], a[4:7], v[192:195]\n"
      "v_exp_f32 v205, v205\n"
      "v_mfma_f32_16x16x32_bf16 v[192:195], a[72:75], a[8:11], v[192:195]\n"
      "ds_read_b64_tr_b16 a[112:113], v210 offset:16896\n"
      "v_cvt_pk_bf16_f32 v190, v204, v205\n"
      "v_mfma_f32_16x16x32_bf16 v[192:195], a[76:79], a[12:15], v[192:195]\n"
      "v_exp_f32 v206, v206\n"
      "v_mfma_f32_16x16x32_bf16 v[196:199], a[76:79], a[28:31], 0\n"
      "ds_read_b128 a[84:87], v209 offset:768\n"
      "v_mfma_f32_16x16x32_bf16 v[196:199], a[72:75], a[24:27], v[196:199]\n"
      "v_exp_f32 v207, v207\n"
      "v_mfma_f32_16x16x32_bf16 v[196:199], a[68:71], a[20:23], v[196:199]\n"
      "ds_read_b64_tr_b16 a[114:115], v211 offset:17152\n"
      "v_cvt_pk_bf16_f32 v191, v206, v207\n"
      "v_mfma_f32_16x16x32_bf16 v[196:199], a[64:67], a[16:19], v[196:199]\n"
      "v_exp_f32 v192, v192\n"
      "v_mfma_f32_16x16x32_bf16 v[200:203], a[64:67], a[32:35], 0\n"
      "ds_read_b128 a[88:91], v209 offset:1280\n"
      "v_mfma_f32_16x16x32_bf16 v[200:203], a[68:71], a[36:39], v[200:203]\n"
      "v_exp_f32 v193, v193\n"
      "v_mfma_f32_16x16x32_bf16 v[200:203], a[72:75], a[40:43], v[200:203]\n"
      "ds_read_b64_tr_b16 a[116:117], v210 offset:25088\n"
      "v_cvt_pk_bf16_f32 v128, v192, v193\n"
      "v_mfma_f32_16x16x32_bf16 v[200:203], a[76:79], a[44:47], v[200:203]\n"
      "v_exp_f32 v194, v194\n"
      "v_mfma_f32_16x16x32_bf16 v[204:207], a[76:79], a[60:63], 0\n"
      "ds_read_b128 a[92:95], v209 offset:1792\n"
      "s_waitcnt lgkmcnt(7)\n"
      "v_mfma_f32_16x16x32_bf16 v[204:207], a[72:75], a[56:59], v[204:207]\n"
      "v_exp_f32 v195, v195\n"
      "v_mfma_f32_16x16x32_bf16 v[204:207], a[68:71], a[52:55], v[204:207]\n"
      "ds_read_b64_tr_b16 a[118:119], v211 offset:25344\n"
      "v_cvt_pk_bf16_f32 v129, v194, v195\n"
      "v_mfma_f32_16x16x32_bf16 v[204:207], a[64:67], a[48:51], v[204:207]\n"
      "v_exp_f32 v196, v196\n"
      "v_mfma_f32_16x16x32_bf16 v[0:3], a[96:99], v[160:163], v[0:3]\n"
      "ds_read_b64_tr_b16 a[120:121], v212 offset:16896\n"
      "v_mfma_f32_16x16x32_bf16 v[0:3], a[100:103], v[164:167], v[0:3]\n"
      "v_exp_f32 v197, v197\n"
      "v_mfma_f32_16x16x32_bf16 v[32:35], a[100:103], v[172:175], v[32:35]\n"
      "ds_read_b64_tr_b16 a[122:123], v213 offset:17152\n"
      "v_cvt_pk_bf16_f32 v136, v196, v197\n"
      "v_mfma_f32_16x16x32_bf16 v[32:35], a[96:99], v[168:171], v[32:35]\n"
      "v_exp_f32 v198, v198\n"
      "v_mfma_f32_16x16x32_bf16 v[64:67], a[96:99], v[176:179], v[64:67]\n"
      "ds_read_b64_tr_b16 a[124:125], v212 offset:25088\n"
      "v_mfma_f32_16x16x32_bf16 v[64:67], a[100:103], v[180:183], v[64:67]\n"
      "v_exp_f32 v199, v199\n"
      "v_mfma_f32_16x16x32_bf16 v[96:99], a[100:103], v[188:191], v[96:99]\n"
      "ds_read_b64_tr_b16 a[126:127], v213 offset:25344\n"
      "v_cvt_pk_bf16_f32 v137, v198, v199\n"
      "v_mfma_f32_16x16x32_bf16 v[96:99], a[96:99], v[184:187], v[96:99]\n"
      "v_exp_f32 v200, v200\n"
      "v_mfma_f32_16x16x32_bf16 v[100:103], a[104:107], v[184:187], v[100:103]\n"
      "buffer_load_dwordx4 v216, s[40:43], s80 offen offset:2048 lds\n"
      "v_mfma_f32_16x16x32_bf16 v[100:103], a[108:111], v[188:191], v[100:103]\n"
      "v_exp_f32 v201, v201\n"
      "v_mfma_f32_16x16x32_bf16 v[68:71], a[108:111], v[180:183], v[68:71]\n"
      "v_cvt_pk_bf16_f32 v144, v200, v201\n"
      "v_mfma_f32_16x16x32_bf16 v[68:71], a[104:107], v[176:179], v[68:71]\n"
      "v_exp_f32 v202, v202\n"
      "v_mfma_f32_16x16x32_bf16 v[36:39], a[104:107], v[168:171], v[36:39]\n"
      "buffer_load_dwordx4 v217, s[40:43], s80 offen offset:3072 lds\n"
      "v_mfma_f32_16x16x32_bf16 v[36:39], a[108:111], v[172:175], v[36:39]\n"
      "v_exp_f32 v203, v203\n"
      "v_mfma_f32_16x16x32_bf16 v[4:7], a[108:111], v[164:167], v[4:7]\n"
      "v_cvt_pk_bf16_f32 v145, v202, v203\n"
      "s_waitcnt lgkmcnt(5)\n"
      "v_mfma_f32_16x16x32_bf16 v[4:7], a[104:107], v[160:163], v[4:7]\n"
      "v_exp_f32 v204, v204\n"
      "v_mfma_f32_16x16x32_bf16 v[192:195], a[80:83], a[0:3], 0\n"
      "ds_read_b128 a[64:67], v208 offset:8192\n"
      "v_mfma_f32_16x16x32_bf16 v[192:195], a[84:87], a[4:7], v[192:195]\n"
      "v_exp_f32 v205, v205\n"
      "v_mfma_f32_16x16x32_bf16 v[192:195], a[88:91], a[8:11], v[192:195]\n"
      "ds_read_b64_tr_b16 a[96:97], v210 offset:17408\n"
      "v_cvt_pk_bf16_f32 v152, v204, v205\n"
      "v_mfma_f32_16x16x32_bf16 v[192:195], a[92:95], a[12:15], v[192:195]\n"
      "v_exp_f32 v206, v206\n"
      "v_mfma_f32_16x16x32_bf16 v[196:199], a[92:95], a[28:31], 0\n"
      "ds_read_b128 a[68:71], v208 offset:8704\n"
      "v_mfma_f32_16x16x32_bf16 v[196:199], a[88:91], a[24:27], v[196:199]\n"
      "v_exp_f32 v207, v207\n"
      "v_mfma_f32_16x16x32_bf16 v[196:199], a[84:87], a[20:23], v[196:199]\n"
      "ds_read_b64_tr_b16 a[98:99], v211 offset:17664\n"
      "v_cvt_pk_bf16_f32 v153, v206, v207\n"
      "v_mfma_f32_16x16x32_bf16 v[196:199], a[80:83], a[16:19], v[196:199]\n"
      "v_exp_f32 v192, v192\n"
      "v_mfma_f32_16x16x32_bf16 v[200:203], a[80:83], a[32:35], 0\n"
      "ds_read_b128 a[72:75], v208 offset:9216\n"
      "v_mfma_f32_16x16x32_bf16 v[200:203], a[84:87], a[36:39], v[200:203]\n"
      "v_exp_f32 v193, v193\n"
      "v_mfma_f32_16x16x32_bf16 v[200:203], a[88:91], a[40:43], v[200:203]\n"
      "ds_read_b64_tr_b16 a[100:101], v210 offset:25600\n"
      "v_cvt_pk_bf16_f32 v130, v192, v193\n"
      "v_mfma_f32_16x16x32_bf16 v[200:203], a[92:95], a[44:47], v[200:203]\n"
      "v_exp_f32 v194, v194\n"
      "v_mfma_f32_16x16x32_bf16 v[204:207], a[92:95], a[60:63], 0\n"
      "ds_read_b128 a[76:79], v208 offset:9728\n"
      "s_waitcnt lgkmcnt(7)\n"
      "v_mfma_f32_16x16x32_bf16 v[204:207], a[88:91], a[56:59], v[204:207]\n"
      "v_exp_f32 v195, v195\n"
      "v_mfma_f32_16x16x32_bf16 v[204:207], a[84:87], a[52:55], v[204:207]\n"
      "ds_read_b64_tr_b16 a[102:103], v211 offset:25856\n"
      "v_cvt_pk_bf16_f32 v131, v194, v195\n"
      "v_mfma_f32_16x16x32_bf16 v[204:207], a[80:83], a[48:51], v[204:207]\n"
      "v_exp_f32 v196, v196\n"
      "v_mfma_f32_16x16x32_bf16 v[8:11], a[112:115], v[160:163], v[8:11]\n"
      "ds_read_b64_tr_b16 a[104:105], v212 offset:17408\n"
      "v_mfma_f32_16x16x32_bf16 v[8:11], a[116:119], v[164:167], v[8:11]\n"
      "v_exp_f32 v197, v197\n"
      "v_mfma_f32_16x16x32_bf16 v[40:43], a[116:119], v[172:175], v[40:43]\n"
      "ds_read_b64_tr_b16 a[106:107], v213 offset:17664\n"
      "v_cvt_pk_bf16_f32 v138, v196, v197\n"
      "v_mfma_f32_16x16x32_bf16 v[40:43], a[112:115], v[168:171], v[40:43]\n"
      "v_exp_f32 v198, v198\n"
      "v_mfma_f32_16x16x32_bf16 v[72:75], a[112:115], v[176:179], v[72:75]\n"
      "ds_read_b64_tr_b16 a[108:109], v212 offset:25600\n"
      "v_mfma_f32_16x16x32_bf16 v[72:75], a[116:119], v[180:183], v[72:75]\n"
      "v_exp_f32 v199, v199\n"
      "v_mfma_f32_16x16x32_bf16 v[104:107], a[116:119], v[188:191], v[104:107]\n"
      "ds_read_b64_tr_b16 a[110:111], v213 offset:25856\n"
      "v_cvt_pk_bf16_f32 v139, v198, v199\n"
      "v_mfma_f32_16x16x32_bf16 v[104:107], a[112:115], v[184:187], v[104:107]\n"
      "v_exp_f32 v200, v200\n"
      "v_mfma_f32_16x16x32_bf16 v[108:111], a[120:123], v[184:187], v[108:111]\n"
      "s_add_u32 s80, s80, 0x4000\n"
      "v_mfma_f32_16x16x32_bf16 v[108:111], a[124:127], v[188:191], v[108:111]\n"
      "v_exp_f32 v201, v201\n"
      "v_mfma_f32_16x16x32_bf16 v[76:79], a[124:127], v[180:183], v[76:79]\n"
      "v_cvt_pk_bf16_f32 v146, v200, v201\n"
      "v_mfma_f32_16x16x32_bf16 v[76:79], a[120:123], v[176:179], v[76:79]\n"
      "v_exp_f32 v202, v202\n"
      "v_mfma_f32_16x16x32_bf16 v[44:47], a[120:123], v[168:171], v[44:47]\n"
      "v_mfma_f32_16x16x32_bf16 v[44:47], a[124:127], v[172:175], v[44:47]\n"
      "v_exp_f32 v203, v203\n"
      "v_mfma_f32_16x16x32_bf16 v[12:15], a[124:127], v[164:167], v[12:15]\n"
      "v_cvt_pk_bf16_f32 v147, v202, v203\n"
      "s_waitcnt lgkmcnt(5)\n"
      "v_mfma_f32_16x16x32_bf16 v[12:15], a[120:123], v[160:163], v[12:15]\n"
      "v_exp_f32 v204, v204\n"
      "v_mfma_f32_16x16x32_bf16 v[192:195], a[64:67], a[0:3], 0\n"
      "ds_read_b128 a[80:83], v209 offset:8448\n"
      "v_mfma_f32_16x16x32_bf16 v[192:195], a[68:71], a[4:7], v[192:195]\n"
      "v_exp_f32 v205, v205\n"
      "v_mfma_f32_16x16x32_bf16 v[192:195], a[72:75], a[8:11], v[192:195]\n"
      "ds_read_b64_tr_b16 a[112:113], v210 offset:17920\n"
      "v_cvt_pk_bf16_f32 v154, v204, v205\n"
      "v_mfma_f32_16x16x32_bf16 v[192:195], a[76:79], a[12:15], v[192:195]\n"
      "v_exp_f32 v206, v206\n"
      "v_mfma_f32_16x16x32_bf16 v[196:199], a[76:79], a[28:31], 0\n"
      "ds_read_b128 a[84:87], v209 offset:8960\n"
      "v_mfma_f32_16x16x32_bf16 v[196:199], a[72:75], a[24:27], v[196:199]\n"
      "v_exp_f32 v207, v207\n"
      "v_mfma_f32_16x16x32_bf16 v[196:199], a[68:71], a[20:23], v[196:199]\n"
      "ds_read_b64_tr_b16 a[114:115], v211 offset:18176\n"
      "v_cvt_pk_bf16_f32 v155, v206, v207\n"
      "v_mfma_f32_16x16x32_bf16 v[196:199], a[64:67], a[16:19], v[196:199]\n"
      "v_exp_f32 v192, v192\n"
      "v_mfma_f32_16x16x32_bf16 v[200:203], a[64:67], a[32:35], 0\n"
      "ds_read_b128 a[88:91], v209 offset:9472\n"
      "v_mfma_f32_16x16x32_bf16 v[200:203], a[68:71], a[36:39], v[200:203]\n"
      "v_exp_f32 v193, v193\n"
      "v_mfma_f32_16x16x32_bf16 v[200:203], a[72:75], a[40:43], v[200:203]\n"
      "ds_read_b64_tr_b16 a[116:117], v210 offset:26112\n"
      "v_cvt_pk_bf16_f32 v132, v192, v193\n"
      "v_mfma_f32_16x16x32_bf16 v[200:203], a[76:79], a[44:47], v[200:203]\n"
      "v_exp_f32 v194, v194\n"
      "v_mfma_f32_16x16x32_bf16 v[204:207], a[76:79], a[60:63], 0\n"
      "ds_read_b128 a[92:95], v209 offset:9984\n"
      "s_waitcnt lgkmcnt(7)\n"
      "v_mfma_f32_16x16x32_bf16 v[204:207], a[72:75], a[56:59], v[204:207]\n"
      "v_exp_f32 v195, v195\n"
      "v_mfma_f32_16x16x32_bf16 v[204:207], a[68:71], a[52:55], v[204:207]\n"
      "ds_read_b64_tr_b16 a[118:119], v211 offset:26368\n"
      "v_cvt_pk_bf16_f32 v133, v194, v195\n"
      "v_mfma_f32_16x16x32_bf16 v[204:207], a[64:67], a[48:51], v[204:207]\n"
      "v_exp_f32 v196, v196\n"
      "v_mfma_f32_16x16x32_bf16 v[16:19], a[96:99], v[160:163], v[16:19]\n"
      "ds_read_b64_tr_b16 a[120:121], v212 offset:17920\n"
      "v_mfma_f32_16x16x32_bf16 v[16:19], a[100:103], v[164:167], v[16:19]\n"
      "v_exp_f32 v197, v197\n"
      "v_mfma_f32_16x16x32_bf16 v[48:51], a[100:103], v[172:175], v[48:51]\n"
      "ds_read_b64_tr_b16 a[122:123], v213 offset:18176\n"
      "v_cvt_pk_bf16_f32 v140, v196, v197\n"
      "v_mfma_f32_16x16x32_bf16 v[48:51], a[96:99], v[168:171], v[48:51]\n"
      "v_exp_f32 v198, v198\n"
      "v_mfma_f32_16x16x32_bf16 v[80:83], a[96:99], v[176:179], v[80:83]\n"
      "ds_read_b64_tr_b16 a[124:125], v212 offset:26112\n"
      "v_mfma_f32_16x16x32_bf16 v[80:83], a[100:103], v[180:183], v[80:83]\n"
      "v_exp_f32 v199, v199\n"
      "v_mfma_f32_16x16x32_bf16 v[112:115], a[100:103], v[188:191], v[112:115]\n"
      "ds_read_b64_tr_b16 a[126:127], v213 offset:26368\n"
      "v_cvt_pk_bf16_f32 v141, v198, v199\n"
      "v_mfma_f32_16x16x32_bf16 v[112:115], a[96:99], v[184:187], v[112:115]\n"
      "v_exp_f32 v200, v200\n"
      "v_mfma_f32_16x16x32_bf16 v[116:119], a[104:107], v[184:187], v[116:119]\n"
      "v_add_u32 v210, s81, v241\n"
      "v_xor_b32 v211, 16, v210\n"
      "v_mfma_f32_16x16x32_bf16 v[116:119], a[108:111], v[188:191], v[116:119]\n"
      "v_exp_f32 v201, v201\n"
      "v_mfma_f32_16x16x32_bf16 v[84:87], a[108:111], v[180:183], v[84:87]\n"
      "v_cvt_pk_bf16_f32 v148, v200, v201\n"
      "v_xor_b32 v212, 32, v210\n"
      "v_mfma_f32_16x16x32_bf16 v[84:87], a[104:107], v[176:179], v[84:87]\n"
      "v_exp_f32 v202, v202\n"
      "v_mfma_f32_16x16x32_bf16 v[52:55], a[104:107], v[168:171], v[52:55]\n"
      "v_mfma_f32_16x16x32_bf16 v[52:55], a[108:111], v[172:175], v[52:55]\n"
      "v_exp_f32 v203, v203\n"
      "v_mfma_f32_16x16x32_bf16 v[20:23], a[108:111], v[164:167], v[20:23]\n"
      "v_cvt_pk_bf16_f32 v149, v202, v203\n"
      "s_waitcnt vmcnt(5) lgkmcnt(0)\n"
      "s_barrier\n"
      "v_mfma_f32_16x16x32_bf16 v[20:23], a[104:107], v[160:163], v[20:23]\n"
      "v_exp_f32 v204, v204\n"
      "v_mfma_f32_16x16x32_bf16 v[192:195], a[80:83], a[0:3], 0\n"
      "ds_read_b128 a[64:67], v208 offset:16384\n"
      "v_xor_b32 v213, 48, v210\n"
      "v_mfma_f32_16x16x32_bf16 v[192:195], a[84:87], a[4:7], v[192:195]\n"
      "v_exp_f32 v205, v205\n"
      "v_mfma_f32_16x16x32_bf16 v[192:195], a[88:91], a[8:11], v[192:195]\n"
      "ds_read_b64_tr_b16 a[96:97], v210 offset:0\n"
      "v_cvt_pk_bf16_f32 v156, v204, v205\n"
      "v_mfma_f32_16x16x32_bf16 v[192:195], a[92:95], a[12:15], v[192:195]\n"
      "v_exp_f32 v206, v206\n"
      "v_mfma_f32_16x16x32_bf16 v[196:199], a[92:95], a[28:31], 0\n"
      "ds_read_b128 a[68:71], v208 offset:16896\n"
      "v_mfma_f32_16x16x32_bf16 v[196:199], a[88:91], a[24:27], v[196:199]\n"
      "v_exp_f32 v207, v207\n"
      "v_mfma_f32_16x16x32_bf16 v[196:199], a[84:87], a[20:23], v[196:199]\n"
      "ds_read_b64_tr_b16 a[98:99], v211 offset:256\n"
      "v_cvt_pk_bf16_f32 v157, v206, v207\n"
      "v_mfma_f32_16x16x32_bf16 v[196:199], a[80:83], a[16:19], v[196:199]\n"
      "v_exp_f32 v192, v192\n"
      "v_mfma_f32_16x16x32_bf16 v[200:203], a[80:83], a[32:35], 0\n"
      "ds_read_b128 a[72:75], v208 offset:17408\n"
      "v_mfma_f32_16x16x32_bf16 v[200:203], a[84:87], a[36:39], v[200:203]\n"
      "v_exp_f32 v193, v193\n"
      "v_mfma_f32_16x16x32_bf16 v[200:203], a[88:91], a[40:43], v[200:203]\n"
      "ds_read_b64_tr_b16 a[100:101], v210 offset:8192\n"
      "v_cvt_pk_bf16_f32 v134, v192, v193\n"
      "v_mfma_f32_16x16x32_bf16 v[200:203], a[92:95], a[44:47], v[200:203]\n"
      "v_exp_f32 v194, v194\n"
      "v_mfma_f32_16x16x32_bf16 v[204:207], a[92:95], a[60:63], 0\n"
      "ds_read_b128 a[76:79], v208 offset:17920\n"
      "v_mfma_f32_16x16x32_bf16 v[204:207], a[88:91], a[56:59], v[204:207]\n"
      "v_exp_f32 v195, v195\n"
      "v_mfma_f32_16x16x32_bf16 v[204:207], a[84:87], a[52:55], v[204:207]\n"
      "ds_read_b64_tr_b16 a[102:103], v211 offset:8448\n"
      "v_cvt_pk_bf16_f32 v135, v194, v195\n"
      "v_mfma_f32_16x16x32_bf16 v[204:207], a[80:83], a[48:51], v[204:207]\n"
      "v_exp_f32 v196, v196\n"
      "v_mfma_f32_16x16x32_bf16 v[24:27], a[112:115], v[160:163], v[24:27]\n"
      "ds_read_b64_tr_b16 a[104:105], v212 offset:0\n"
      "v_mfma_f32_16x16x32_bf16 v[24:27], a[116:119], v[164:167], v[24:27]\n"
      "v_exp_f32 v197, v197\n"
      "v_mfma_f32_16x16x32_bf16 v[56:59], a[116:119], v[172:175], v[56:59]\n"
      "ds_read_b64_tr_b16 a[106:107], v213 offset:256\n"
      "v_cvt_pk_bf16_f32 v142, v196, v197\n"
      "v_mfma_f32_16x16x32_bf16 v[56:59], a[112:115], v[168:171], v[56:59]\n"
      "v_exp_f32 v198, v198\n"
      "v_mfma_f32_16x16x32_bf16 v[88:91], a[112:115], v[176:179], v[88:91]\n"
      "ds_read_b64_tr_b16 a[108:109], v212 offset:8192\n"
      "s_mov_b32 m0, s87\n"
      "v_mfma_f32_16x16x32_bf16 v[88:91], a[116:119], v[180:183], v[88:91]\n"
      "v_exp_f32 v199, v199\n"
      "v_mfma_f32_16x16x32_bf16 v[120:123], a[116:119], v[188:191], v[120:123]\n"
      "ds_read_b64_tr_b16 a[110:111], v213 offset:8448\n"
      "v_cvt_pk_bf16_f32 v143, v198, v199\n"
      "v_mfma_f32_16x16x32_bf16 v[120:123], a[112:115], v[184:187], v[120:123]\n"
      "v_exp_f32 v200, v200\n"
      "v_mfma_f32_16x16x32_bf16 v[124:127], a[120:123], v[184:187], v[124:127]\n"
      "buffer_load_dwordx4 v214, s[40:43], s80 offen offset:0 lds\n"
      "s_mov_b32 s87, s81\n"
      "v_mfma_f32_16x16x32_bf16 v[124:127], a[124:127], v[188:191], v[124:127]\n"
      "v_exp_f32 v201, v201\n"
      "v_mfma_f32_16x16x32_bf16 v[92:95], a[124:127], v[180:183], v[92:95]\n"
      "v_cvt_pk_bf16_f32 v150, v200, v201\n"
      "s_mov_b32 s81, s86\n"
      "v_mfma_f32_16x16x32_bf16 v[92:95], a[120:123], v[176:179], v[92:95]\n"
      "v_exp_f32 v202, v202\n"
      "v_mfma_f32_16x16x32_bf16 v[60:63], a[120:123], v[168:171], v[60:63]\n"
      "buffer_load_dwordx4 v215, s[40:43], s80 offen offset:1024 lds\n"
      "s_mov_b32 s86, m0\n"
      "v_mfma_f32_16x16x32_bf16 v[60:63], a[124:127], v[172:175], v[60:63]\n"
      "v_exp_f32 v203, v203\n"
      "v_mfma_f32_16x16x32_bf16 v[28:31], a[124:127], v[164:167], v[28:31]\n"
      "v_cvt_pk_bf16_f32 v151, v202, v203\n"
      "s_waitcnt lgkmcnt(5)\n"
      "v_mfma_f32_16x16x32_bf16 v[28:31], a[120:123], v[160:163], v[28:31]\n"
      "v_exp_f32 v204, v204\n"
      "s_sub_u32 s82, s82, 1\n"
      "s_cbranch_scc0 2b\n"
      "v_mfma_f32_16x16x32_bf16 v[192:195], a[64:67], a[0:3], 0\n"
      "ds_read_b128 a[80:83], v209 offset:16640\n"
      "v_mfma_f32_16x16x32_bf16 v[192:195], a[68:71], a[4:7], v[192:195]\n"
      "v_exp_f32 v205, v205\n"
      "v_mfma_f32_16x16x32_bf16 v[192:195], a[72:75], a[8:11], v[192:195]\n"
      "ds_read_b64_tr_b16 a[112:113], v210 offset:512\n"
      "v_cvt_pk_bf16_f32 v158, v204, v205\n"
      "v_mfma_f32_16x16x32_bf16 v[192:195], a[76:79], a[12:15], v[192:195]\n"
      "v_exp_f32 v206, v206\n"
      "v_mfma_f32_16x16x32_bf16 v[196:199], a[76:79], a[28:31], 0\n"
      "ds_read_b128 a[84:87], v209 offset:17152\n"
      "v_mfma_f32_16x16x32_bf16 v[196:199], a[72:75], a[24:27], v[196:199]\n"
      "v_exp_f32 v207, v207\n"
      "v_mfma_f32_16x16x32_bf16 v[196:199], a[68:71], a[20:23], v[196:199]\n"
      "ds_read_b64_tr_b16 a[114:115], v211 offset:768\n"
      "v_cvt_pk_bf16_f32 v159, v206, v207\n"
      "v_mfma_f32_16x16x32_bf16 v[196:199], a[64:67], a[16:19], v[196:199]\n"
      "v_exp_f32 v192, v192\n"
      "v_mfma_f32_16x16x32_bf16 v[200:203], a[64:67], a[32:35], 0\n"
      "ds_read_b128 a[88:91], v209 offset:17664\n"
      "v_mfma_f32_16x16x32_bf16 v[200:203], a[68:71], a[36:39], v[200:203]\n"
      "v_exp_f32 v193, v193\n"
      "v_mfma_f32_16x16x32_bf16 v[200:203], a[72:75], a[40:43], v[200:203]\n"
      "ds_read_b64_tr_b16 a[116:117], v210 offset:8704\n"
      "v_cvt_pk_bf16_f32 v160, v192, v193\n"
      "v_mfma_f32_16x16x32_bf16 v[200:203], a[76:79], a[44:47], v[200:203]\n"
      "v_exp_f32 v194, v194\n"
      "v_mfma_f32_16x16x32_bf16 v[204:207], a[76:79], a[60:63], 0\n"
      "ds_read_b128 a[92:95], v209 offset:18176\n"
      "s_waitcnt lgkmcnt(7)\n"
      "v_mfma_f32_16x16x32_bf16 v[204:207], a[72:75], a[56:59], v[204:207]\n"
      "v_exp_f32 v195, v195\n"
      "v_mfma_f32_16x16x32_bf16 v[204:207], a[68:71], a[52:55], v[204:207]\n"
      "ds_read_b64_tr_b16 a[118:119], v211 offset:8960\n"
      "v_cvt_pk_bf16_f32 v161, v194, v195\n"
      "v_mfma_f32_16x16x32_bf16 v[204:207], a[64:67], a[48:51], v[204:207]\n"
      "v_exp_f32 v196, v196\n"
      "v_mfma_f32_16x16x32_bf16 v[0:3], a[96:99], v[128:131], v[0:3]\n"
      "ds_read_b64_tr_b16 a[120:121], v212 offset:512\n"
      "v_mfma_f32_16x16x32_bf16 v[0:3], a[100:103], v[132:135], v[0:3]\n"
      "v_exp_f32 v197, v197\n"
      "v_mfma_f32_16x16x32_bf16 v[32:35], a[100:103], v[140:143], v[32:35]\n"
      "ds_read_b64_tr_b16 a[122:123], v213 offset:768\n"
      "v_cvt_pk_bf16_f32 v168, v196, v197\n"
      "v_mfma_f32_16x16x32_bf16 v[32:35], a[96:99], v[136:139], v[32:35]\n"
      "v_exp_f32 v198, v198\n"
      "v_mfma_f32_16x16x32_bf16 v[64:67], a[96:99], v[144:147], v[64:67]\n"
      "ds_read_b64_tr_b16 a[124:125], v212 offset:8704\n"
      "v_mfma_f32_16x16x32_bf16 v[64:67], a[100:103], v[148:151], v[64:67]\n"
      "v_exp_f32 v199, v199\n"
      "v_mfma_f32_16x16x32_bf16 v[96:99], a[100:103], v[156:159], v[96:99]\n"
      "ds_read_b64_tr_b16 a[126:127], v213 offset:8960\n"
      "v_cvt_pk_bf16_f32 v169, v198, v199\n"
      "v_mfma_f32_16x16x32_bf16 v[96:99], a[96:99], v[152:155], v[96:99]\n"
      "v_exp_f32 v200, v200\n"
      "v_mfma_f32_16x16x32_bf16 v[100:103], a[104:107], v[152:155], v[100:103]\n"
      "buffer_load_dwordx4 v216, s[40:43], s80 offen offset:2048 lds\n"
      "v_mfma_f32_16x16x32_bf16 v[100:103], a[108:111], v[156:159], v[100:103]\n"
      "v_exp_f32 v201, v201\n"
      "v_mfma_f32_16x16x32_bf16 v[68:71], a[108:111], v[148:151], v[68:71]\n"
      "v_cvt_pk_bf16_f32 v176, v200, v201\n"
      "v_mfma_f32_16x16x32_bf16 v[68:71], a[104:107], v[144:147], v[68:71]\n"
      "v_exp_f32 v202, v202\n"
      "v_mfma_f32_16x16x32_bf16 v[36:39], a[104:107], v[136:139], v[36:39]\n"
      "buffer_load_dwordx4 v217, s[40:43], s80 offen offset:3072 lds\n"
      "v_mfma_f32_16x16x32_bf16 v[36:39], a[108:111], v[140:143], v[36:39]\n"
      "v_exp_f32 v203, v203\n"
      "v_mfma_f32_16x16x32_bf16 v[4:7], a[108:111], v[132:135], v[4:7]\n"
      "v_cvt_pk_bf16_f32 v177, v202, v203\n"
      "s_waitcnt lgkmcnt(5)\n"
      "v_mfma_f32_16x16x32_bf16 v[4:7], a[104:107], v[128:131], v[4:7]\n"
      "v_exp_f32 v204, v204\n"
      "v_mfma_f32_16x16x32_bf16 v[192:195], a[80:83], a[0:3], 0\n"
      "ds_read_b128 a[64:67], v208 offset:24576\n"
      "s_add_u32 m0, s65, 0x24000\n"
      "v_mfma_f32_16x16x32_bf16 v[192:195], a[84:87], a[4:7], v[192:195]\n"
      "v_exp_f32 v205, v205\n"
      "v_mfma_f32_16x16x32_bf16 v[192:195], a[88:91], a[8:11], v[192:195]\n"
      "ds_read_b64_tr_b16 a[96:97], v210 offset:1024\n"
      "v_cvt_pk_bf16_f32 v184, v204, v205\n"
      "v_mfma_f32_16x16x32_bf16 v[192:195], a[92:95], a[12:15], v[192:195]\n"
      "v_exp_f32 v206, v206\n"
      "v_mfma_f32_16x16x32_bf16 v[196:199], a[92:95], a[28:31], 0\n"
      "ds_read_b128 a[68:71], v208 offset:25088\n"
      "s_mov_b32 s85, 3260416\n"
      "v_mfma_f32_16x16x32_bf16 v[196:199], a[88:91], a[24:27], v[196:199]\n"
      "v_exp_f32 v207, v207\n"
      "v_mfma_f32_16x16x32_bf16 v[196:199], a[84:87], a[20:23], v[196:199]\n"
      "ds_read_b64_tr_b16 a[98:99], v211 offset:1280\n"
      "v_cvt_pk_bf16_f32 v185, v206, v207\n"
      "v_mfma_f32_16x16x32_bf16 v[196:199], a[80:83], a[16:19], v[196:199]\n"
      "v_exp_f32 v192, v192\n"
      "v_mfma_f32_16x16x32_bf16 v[200:203], a[80:83], a[32:35], 0\n"
      "ds_read_b128 a[72:75], v208 offset:25600\n"
      "buffer_load_dwordx4 v214, s[40:43], s85 offen offset:0 lds\n"
      "v_mfma_f32_16x16x32_bf16 v[200:203], a[84:87], a[36:39], v[200:203]\n"
      "v_exp_f32 v193, v193\n"
      "v_mfma_f32_16x16x32_bf16 v[200:203], a[88:91], a[40:43], v[200:203]\n"
      "ds_read_b64_tr_b16 a[100:101], v210 offset:9216\n"
      "v_cvt_pk_bf16_f32 v162, v192, v193\n"
      "v_mfma_f32_16x16x32_bf16 v[200:203], a[92:95], a[44:47], v[200:203]\n"
      "v_exp_f32 v194, v194\n"
      "v_mfma_f32_16x16x32_bf16 v[204:207], a[92:95], a[60:63], 0\n"
      "ds_read_b128 a[76:79], v208 offset:26112\n"
      "s_waitcnt lgkmcnt(7)\n"
      "v_mfma_f32_16x16x32_bf16 v[204:207], a[88:91], a[56:59], v[204:207]\n"
      "v_exp_f32 v195, v195\n"
      "v_mfma_f32_16x16x32_bf16 v[204:207], a[84:87], a[52:55], v[204:207]\n"
      "ds_read_b64_tr_b16 a[102:103], v211 offset:9472\n"
      "v_cvt_pk_bf16_f32 v163, v194, v195\n"
      "v_mfma_f32_16x16x32_bf16 v[204:207], a[80:83], a[48:51], v[204:207]\n"
      "v_exp_f32 v196, v196\n"
      "v_mfma_f32_16x16x32_bf16 v[8:11], a[112:115], v[128:131], v[8:11]\n"
      "ds_read_b64_tr_b16 a[104:105], v212 offset:1024\n"
      "buffer_load_dwordx4 v215, s[40:43], s85 offen offset:1024 lds\n"
      "v_mfma_f32_16x16x32_bf16 v[8:11], a[116:119], v[132:135], v[8:11]\n"
      "v_exp_f32 v197, v197\n"
      "v_mfma_f32_16x16x32_bf16 v[40:43], a[116:119], v[140:143], v[40:43]\n"
      "ds_read_b64_tr_b16 a[106:107], v213 offset:1280\n"
      "v_cvt_pk_bf16_f32 v170, v196, v197\n"
      "v_mfma_f32_16x16x32_bf16 v[40:43], a[112:115], v[136:139], v[40:43]\n"
      "v_exp_f32 v198, v198\n"
      "v_mfma_f32_16x16x32_bf16 v[72:75], a[112:115], v[144:147], v[72:75]\n"
      "ds_read_b64_tr_b16 a[108:109], v212 offset:9216\n"
      "v_mfma_f32_16x16x32_bf16 v[72:75], a[116:119], v[148:151], v[72:75]\n"
      "v_exp_f32 v199, v199\n"
      "v_mfma_f32_16x16x32_bf16 v[104:107], a[116:119], v[156:159], v[104:107]\n"
      "ds_read_b64_tr_b16 a[110:111], v213 offset:9472\n"
      "v_cvt_pk_bf16_f32 v171, v198, v199\n"
      "v_mfma_f32_16x16x32_bf16 v[104:107], a[112:115], v[152:155], v[104:107]\n"
      "v_exp_f32 v200, v200\n"
      "v_mfma_f32_16x16x32_bf16 v[108:111], a[120:123], v[152:155], v[108:111]\n"
      "s_add_u32 s80, s80, 0x4000\n"
      "v_mfma_f32_16x16x32_bf16 v[108:111], a[124:127], v[156:159], v[108:111]\n"
      "v_exp_f32 v201, v201\n"
      "v_mfma_f32_16x16x32_bf16 v[76:79], a[124:127], v[148:151], v[76:79]\n"
      "v_cvt_pk_bf16_f32 v178, v200, v201\n"
      "s_cmp_eq_u32 s80, s89\n"
      "v_mfma_f32_16x16x32_bf16 v[76:79], a[120:123], v[144:147], v[76:79]\n"
      "v_exp_f32 v202, v202\n"
      "v_mfma_f32_16x16x32_bf16 v[44:47], a[120:123], v[136:139], v[44:47]\n"
      "s_cselect_b32 s80, s92, s80\n"
      "v_mfma_f32_16x16x32_bf16 v[44:47], a[124:127], v[140:143], v[44:47]\n"
      "v_exp_f32 v203, v203\n"
      "v_mfma_f32_16x16x32_bf16 v[12:15], a[124:127], v[132:135], v[12:15]\n"
      "v_cvt_pk_bf16_f32 v179, v202, v203\n"
      "s_waitcnt lgkmcnt(5)\n"
      "v_mfma_f32_16x16x32_bf16 v[12:15], a[120:123], v[128:131], v[12:15]\n"
      "v_exp_f32 v204, v204\n"
      "v_mfma_f32_16x16x32_bf16 v[192:195], a[64:67], a[0:3], 0\n"
      "ds_read_b128 a[80:83], v209 offset:24832\n"
      "v_mfma_f32_16x16x32_bf16 v[192:195], a[68:71], a[4:7], v[192:195]\n"
      "v_exp_f32 v205, v205\n"
      "v_mfma_f32_16x16x32_bf16 v[192:195], a[72:75], a[8:11], v[192:195]\n"
      "ds_read_b64_tr_b16 a[112:113], v210 offset:1536\n"
      "v_cvt_pk_bf16_f32 v186, v204, v205\n"
      "v_mfma_f32_16x16x32_bf16 v[192:195], a[76:79], a[12:15], v[192:195]\n"
      "v_exp_f32 v206, v206\n"
      "v_mfma_f32_16x16x32_bf16 v[196:199], a[76:79], a[28:31], 0\n"
      "ds_read_b128 a[84:87], v209 offset:25344\n"
      "v_mfma_f32_16x16x32_bf16 v[196:199], a[72:75], a[24:27], v[196:199]\n"
      "v_exp_f32 v207, v207\n"
      "v_mfma_f32_16x16x32_bf16 v[196:199], a[68:71], a[20:23], v[196:199]\n"
      "ds_read_b64_tr_b16 a[114:115], v211 offset:1792\n"
      "v_cvt_pk_bf16_f32 v187, v206, v207\n"
      "v_mfma_f32_16x16x32_bf16 v[196:199], a[64:67], a[16:19], v[196:199]\n"
      "v_exp_f32 v192, v192\n"
      "v_mfma_f32_16x16x32_bf16 v[200:203], a[64:67], a[32:35], 0\n"
      "ds_read_b128 a[88:91], v209 offset:25856\n"
      "v_mfma_f32_16x16x32_bf16 v[200:203], a[68:71], a[36:39], v[200:203]\n"
      "v_exp_f32 v193, v193\n"
      "v_mfma_f32_16x16x32_bf16 v[200:203], a[72:75], a[40:43], v[200:203]\n"
      "ds_read_b64_tr_b16 a[116:117], v210 offset:9728\n"
      "v_cvt_pk_bf16_f32 v164, v192, v193\n"
      "v_mfma_f32_16x16x32_bf16 v[200:203], a[76:79], a[44:47], v[200:203]\n"
      "v_exp_f32 v194, v194\n"
      "v_mfma_f32_16x16x32_bf16 v[204:207], a[76:79], a[60:63], 0\n"
      "ds_read_b128 a[92:95], v209 offset:26368\n"
      "s_waitcnt lgkmcnt(7)\n"
      "v_mfma_f32_16x16x32_bf16 v[204:207], a[72:75], a[56:59], v[204:207]\n"
      "v_exp_f32 v195, v195\n"
      "v_mfma_f32_16x16x32_bf16 v[204:207], a[68:71], a[52:55], v[204:207]\n"
      "ds_read_b64_tr_b16 a[118:119], v211 offset:9984\n"
      "v_cvt_pk_bf16_f32 v165, v194, v195\n"
      "v_mfma_f32_16x16x32_bf16 v[204:207], a[64:67], a[48:51], v[204:207]\n"
      "v_exp_f32 v196, v196\n"
      "v_mfma_f32_16x16x32_bf16 v[16:19], a[96:99], v[128:131], v[16:19]\n"
      "ds_read_b64_tr_b16 a[120:121], v212 offset:1536\n"
      "v_add_u32 v208, s81, v240\n"
      "v_mfma_f32_16x16x32_bf16 v[16:19], a[100:103], v[132:135], v[16:19]\n"
      "v_exp_f32 v197, v197\n"
      "v_mfma_f32_16x16x32_bf16 v[48:51], a[100:103], v[140:143], v[48:51]\n"
      "ds_read_b64_tr_b16 a[122:123], v213 offset:1792\n"
      "v_cvt_pk_bf16_f32 v172, v196, v197\n"
      "v_mfma_f32_16x16x32_bf16 v[48:51], a[96:99], v[136:139], v[48:51]\n"
      "v_exp_f32 v198, v198\n"
      "v_mfma_f32_16x16x32_bf16 v[80:83], a[96:99], v[144:147], v[80:83]\n"
      "ds_read_b64_tr_b16 a[124:125], v212 offset:9728\n"
      "v_mfma_f32_16x16x32_bf16 v[80:83], a[100:103], v[148:151], v[80:83]\n"
      "v_exp_f32 v199, v199\n"
      "v_mfma_f32_16x16x32_bf16 v[112:115], a[100:103], v[156:159], v[112:115]\n"
      "ds_read_b64_tr_b16 a[126:127], v213 offset:9984\n"
      "v_cvt_pk_bf16_f32 v173, v198, v199\n"
      "v_mfma_f32_16x16x32_bf16 v[112:115], a[96:99], v[152:155], v[112:115]\n"
      "v_exp_f32 v200, v200\n"
      "v_mfma_f32_16x16x32_bf16 v[116:119], a[104:107], v[152:155], v[116:119]\n"
      "buffer_load_dwordx4 v216, s[40:43], s85 offen offset:2048 lds\n"
      "v_mfma_f32_16x16x32_bf16 v[116:119], a[108:111], v[156:159], v[116:119]\n"
      "v_exp_f32 v201, v201\n"
      "v_mfma_f32_16x16x32_bf16 v[84:87], a[108:111], v[148:151], v[84:87]\n"
      "v_cvt_pk_bf16_f32 v180, v200, v201\n"
      "v_xor_b32 v209, 16, v208\n"
      "v_mfma_f32_16x16x32_bf16 v[84:87], a[104:107], v[144:147], v[84:87]\n"
      "v_exp_f32 v202, v202\n"
      "v_mfma_f32_16x16x32_bf16 v[52:55], a[104:107], v[136:139], v[52:55]\n"
      "buffer_load_dwordx4 v217, s[40:43], s85 offen offset:3072 lds\n"
      "v_mfma_f32_16x16x32_bf16 v[52:55], a[108:111], v[140:143], v[52:55]\n"
      "v_exp_f32 v203, v203\n"
      "v_mfma_f32_16x16x32_bf16 v[20:23], a[108:111], v[132:135], v[20:23]\n"
      "v_cvt_pk_bf16_f32 v181, v202, v203\n"
      "s_waitcnt lgkmcnt(5)\n"
      "v_mfma_f32_16x16x32_bf16 v[20:23], a[104:107], v[128:131], v[20:23]\n"
      "v_exp_f32 v204, v204\n"
      "v_mfma_f32_16x16x32_bf16 v[192:195], a[80:83], a[0:3], 0\n"
      "ds_read_b128 a[64:67], v208 offset:0\n"
      "v_mfma_f32_16x16x32_bf16 v[192:195], a[84:87], a[4:7], v[192:195]\n"
      "v_exp_f32 v205, v205\n"
      "v_mfma_f32_16x16x32_bf16 v[192:195], a[88:91], a[8:11], v[192:195]\n"
      "ds_read_b64_tr_b16 a[96:97], v210 offset:16384\n"
      "v_cvt_pk_bf16_f32 v188, v204, v205\n"
      "v_mfma_f32_16x16x32_bf16 v[192:195], a[92:95], a[12:15], v[192:195]\n"
      "v_exp_f32 v206, v206\n"
      "v_mfma_f32_16x16x32_bf16 v[196:199], a[92:95], a[28:31], 0\n"
      "ds_read_b128 a[68:71], v208 offset:512\n"
      "v_mfma_f32_16x16x32_bf16 v[196:199], a[88:91], a[24:27], v[196:199]\n"
      "v_exp_f32 v207, v207\n"
      "v_mfma_f32_16x16x32_bf16 v[196:199], a[84:87], a[20:23], v[196:199]\n"
      "ds_read_b64_tr_b16 a[98:99], v211 offset:16640\n"
      "v_cvt_pk_bf16_f32 v189, v206, v207\n"
      "v_mfma_f32_16x16x32_bf16 v[196:199], a[80:83], a[16:19], v[196:199]\n"
      "v_exp_f32 v192, v192\n"
      "v_mfma_f32_16x16x32_bf16 v[200:203], a[80:83], a[32:35], 0\n"
      "ds_read_b128 a[72:75], v208 offset:1024\n"
      "v_mfma_f32_16x16x32_bf16 v[200:203], a[84:87], a[36:39], v[200:203]\n"
      "v_exp_f32 v193, v193\n"
      "v_mfma_f32_16x16x32_bf16 v[200:203], a[88:91], a[40:43], v[200:203]\n"
      "ds_read_b64_tr_b16 a[100:101], v210 offset:24576\n"
      "v_cvt_pk_bf16_f32 v166, v192, v193\n"
      "v_mfma_f32_16x16x32_bf16 v[200:203], a[92:95], a[44:47], v[200:203]\n"
      "v_exp_f32 v194, v194\n"
      "v_mfma_f32_16x16x32_bf16 v[204:207], a[92:95], a[60:63], 0\n"
      "ds_read_b128 a[76:79], v208 offset:1536\n"
      "s_waitcnt lgkmcnt(7)\n"
      "v_mfma_f32_16x16x32_bf16 v[204:207], a[88:91], a[56:59], v[204:207]\n"
      "v_exp_f32 v195, v195\n"
      "v_mfma_f32_16x16x32_bf16 v[204:207], a[84:87], a[52:55], v[204:207]\n"
      "ds_read_b64_tr_b16 a[102:103], v211 offset:24832\n"
      "v_cvt_pk_bf16_f32 v167, v194, v195\n"
      "v_mfma_f32_16x16x32_bf16 v[204:207], a[80:83], a[48:51], v[204:207]\n"
      "v_exp_f32 v196, v196\n"
      "v_mfma_f32_16x16x32_bf16 v[24:27], a[112:115], v[128:131], v[24:27]\n"
      "ds_read_b64_tr_b16 a[104:105], v212 offset:16384\n"
      "v_mfma_f32_16x16x32_bf16 v[24:27], a[116:119], v[132:135], v[24:27]\n"
      "v_exp_f32 v197, v197\n"
      "v_mfma_f32_16x16x32_bf16 v[56:59], a[116:119], v[140:143], v[56:59]\n"
      "ds_read_b64_tr_b16 a[106:107], v213 offset:16640\n"
      "v_cvt_pk_bf16_f32 v174, v196, v197\n"
      "v_mfma_f32_16x16x32_bf16 v[56:59], a[112:115], v[136:139], v[56:59]\n"
      "v_exp_f32 v198, v198\n"
      "v_mfma_f32_16x16x32_bf16 v[88:91], a[112:115], v[144:147], v[88:91]\n"
      "ds_read_b64_tr_b16 a[108:109], v212 offset:24576\n"
      "s_add_u32 m0, s86, 0x4000\n"
      "v_mfma_f32_16x16x32_bf16 v[88:91], a[116:119], v[148:151], v[88:91]\n"
      "v_exp_f32 v199, v199\n"
      "v_mfma_f32_16x16x32_bf16 v[120:123], a[116:119], v[156:159], v[120:123]\n"
      "ds_read_b64_tr_b16 a[110:111], v213 offset:24832\n"
      "v_cvt_pk_bf16_f32 v175, v198, v199\n"
      "v_mfma_f32_16x16x32_bf16 v[120:123], a[112:115], v[152:155], v[120:123]\n"
      "v_exp_f32 v200, v200\n"
      "v_mfma_f32_16x16x32_bf16 v[124:127], a[120:123], v[152:155], v[124:127]\n"
      "buffer_load_dwordx4 v214, s[40:43], s80 offen offset:0 lds\n"
      "v_mfma_f32_16x16x32_bf16 v[124:127], a[124:127], v[156:159], v[124:127]\n"
      "v_exp_f32 v201, v201\n"
      "v_mfma_f32_16x16x32_bf16 v[92:95], a[124:127], v[148:151], v[92:95]\n"
      "v_cvt_pk_bf16_f32 v182, v200, v201\n"
      "v_mfma_f32_16x16x32_bf16 v[92:95], a[120:123], v[144:147], v[92:95]\n"
      "v_exp_f32 v202, v202\n"
      "v_mfma_f32_16x16x32_bf16 v[60:63], a[120:123], v[136:139], v[60:63]\n"
      "buffer_load_dwordx4 v215, s[40:43], s80 offen offset:1024 lds\n"
      "v_mfma_f32_16x16x32_bf16 v[60:63], a[124:127], v[140:143], v[60:63]\n"
      "v_exp_f32 v203, v203\n"
      "v_mfma_f32_16x16x32_bf16 v[28:31], a[124:127], v[132:135], v[28:31]\n"
      "v_cvt_pk_bf16_f32 v183, v202, v203\n"
      "s_waitcnt lgkmcnt(5)\n"
      "v_mfma_f32_16x16x32_bf16 v[28:31], a[120:123], v[128:131], v[28:31]\n"
      "v_exp_f32 v204, v204\n"
      "v_mfma_f32_16x16x32_bf16 v[192:195], a[64:67], a[0:3], 0\n"
      "v_exp_f32 v205, v205\n"
      "v_mfma_f32_16x16x32_bf16 v[192:195], a[68:71], a[4:7], v[192:195]\n"
      "v_exp_f32 v206, v206\n"
      "v_mfma_f32_16x16x32_bf16 v[192:195], a[72:75], a[8:11], v[192:195]\n"
      "v_exp_f32 v207, v207\n"
      "v_mfma_f32_16x16x32_bf16 v[192:195], a[76:79], a[12:15], v[192:195]\n"
      "ds_read_b128 a[80:83], v209 offset:256\n"
      "v_cvt_pk_bf16_f32 v190, v204, v205\n"
      "v_mfma_f32_16x16x32_bf16 v[196:199], a[76:79], a[28:31], 0\n"
      "ds_read_b128 a[84:87], v209 offset:768\n"
      "v_cvt_pk_bf16_f32 v191, v206, v207\n"
      "v_mfma_f32_16x16x32_bf16 v[196:199], a[72:75], a[24:27], v[196:199]\n"
      "ds_read_b128 a[88:91], v209 offset:1280\n"
      "ds_read_b128 a[92:95], v209 offset:1792\n"
      "v_mfma_f32_16x16x32_bf16 v[196:199], a[68:71], a[20:23], v[196:199]\n"
      "buffer_load_dwordx4 v216, s[40:43], s80 offen offset:2048 lds\n"
      "buffer_load_dwordx4 v217, s[40:43], s80 offen offset:3072 lds\n"
      "v_mfma_f32_16x16x32_bf16 v[196:199], a[64:67], a[16:19], v[196:199]\n"
      "v_exp_f32 v192, v192\n"
      "v_mfma_f32_16x16x32_bf16 v[200:203], a[64:67], a[32:35], 0\n"
      "v_exp_f32 v193, v193\n"
      "s_add_u32 s80, s80, 0x4000\n"
      "v_mfma_f32_16x16x32_bf16 v[200:203], a[68:71], a[36:39], v[200:203]\n"
      "v_exp_f32 v194, v194\n"
      "v_cvt_pk_bf16_f32 v128, v192, v193\n"
      "v_mfma_f32_16x16x32_bf16 v[200:203], a[72:75], a[40:43], v[200:203]\n"
      "v_exp_f32 v195, v195\n"
      "s_waitcnt vmcnt(12)\n"
      "s_barrier\n"
      "v_mfma_f32_16x16x32_bf16 v[200:203], a[76:79], a[44:47], v[200:203]\n"
      "v_exp_f32 v196, v196\n"
      "v_cvt_pk_bf16_f32 v129, v194, v195\n"
      "v_mfma_f32_16x16x32_bf16 v[204:207], a[76:79], a[60:63], 0\n"
      "v_exp_f32 v197, v197\n"
      "s_add_u32 m0, s65, 0x8000\n"
      "v_mfma_f32_16x16x32_bf16 v[204:207], a[72:75], a[56:59], v[204:207]\n"
      "v_exp_f32 v198, v198\n"
      "v_cvt_pk_bf16_f32 v136, v196, v197\n"
      "v_mfma_f32_16x16x32_bf16 v[204:207], a[68:71], a[52:55], v[204:207]\n"
      "buffer_load_dwordx4 v214, s[40:43], s80 offen offset:0 lds\n"
      "v_exp_f32 v199, v199\n"
      "v_mfma_f32_16x16x32_bf16 v[204:207], a[64:67], a[48:51], v[204:207]\n"
      "v_exp_f32 v200, v200\n"
      "v_cvt_pk_bf16_f32 v137, v198, v199\n"
      "s_waitcnt lgkmcnt(0)\n"
      "v_mfma_f32_16x16x32_bf16 v[192:195], a[80:83], a[0:3], 0\n"
      "ds_read_b128 a[64:67], v208 offset:8192\n"
      "v_exp_f32 v201, v201\n"
      "v_mfma_f32_16x16x32_bf16 v[192:195], a[84:87], a[4:7], v[192:195]\n"
      "v_exp_f32 v202, v202\n"
      "v_cvt_pk_bf16_f32 v144, v200, v201\n"
      "v_mfma_f32_16x16x32_bf16 v[192:195], a[88:91], a[8:11], v[192:195]\n"
      "ds_read_b128 a[68:71], v208 offset:8704\n"
      "v_exp_f32 v203, v203\n"
      "v_mfma_f32_16x16x32_bf16 v[192:195], a[92:95], a[12:15], v[192:195]\n"
      "v_exp_f32 v204, v204\n"
      "v_cvt_pk_bf16_f32 v145, v202, v203\n"
      "v_mfma_f32_16x16x32_bf16 v[196:199], a[92:95], a[28:31], 0\n"
      "ds_read_b128 a[72:75], v208 offset:9216\n"
      "v_exp_f32 v205, v205\n"
      "v_mfma_f32_16x16x32_bf16 v[196:199], a[88:91], a[24:27], v[196:199]\n"
      "v_exp_f32 v206, v206\n"
      "v_cvt_pk_bf16_f32 v152, v204, v205\n"
      "v_mfma_f32_16x16x32_bf16 v[196:199], a[84:87], a[20:23], v[196:199]\n"
      "ds_read_b128 a[76:79], v208 offset:9728\n"
      "v_exp_f32 v207, v207\n"
      "v_mfma_f32_16x16x32_bf16 v[196:199], a[80:83], a[16:19], v[196:199]\n"
      "v_exp_f32 v192, v192\n"
      "v_cvt_pk_bf16_f32 v153, v206, v207\n"
      "v_mfma_f32_16x16x32_bf16 v[200:203], a[80:83], a[32:35], 0\n"
      "v_exp_f32 v193, v193\n"
      "v_mfma_f32_16x16x32_bf16 v[200:203], a[84:87], a[36:39], v[200:203]\n"
      "v_exp_f32 v194, v194\n"
      "v_cvt_pk_bf16_f32 v130, v192, v193\n"
      "v_mfma_f32_16x16x32_bf16 v[200:203], a[88:91], a[40:43], v[200:203]\n"
      "buffer_load_dwordx4 v215, s[40:43], s80 offen offset:1024 lds\n"
      "v_exp_f32 v195, v195\n"
      "v_mfma_f32_16x16x32_bf16 v[200:203], a[92:95], a[44:47], v[200:203]\n"
      "v_exp_f32 v196, v196\n"
      "v_cvt_pk_bf16_f32 v131, v194, v195\n"
      "v_mfma_f32_16x16x32_bf16 v[204:207], a[92:95], a[60:63], 0\n"
      "v_exp_f32 v197, v197\n"
      "v_mfma_f32_16x16x32_bf16 v[204:207], a[88:91], a[56:59], v[204:207]\n"
      "v_exp_f32 v198, v198\n"
      "v_cvt_pk_bf16_f32 v138, v196, v197\n"
      "v_mfma_f32_16x16x32_bf16 v[204:207], a[84:87], a[52:55], v[204:207]\n"
      "buffer_load_dwordx4 v216, s[40:43], s80 offen offset:2048 lds\n"
      "v_exp_f32 v199, v199\n"
      "v_mfma_f32_16x16x32_bf16 v[204:207], a[80:83], a[48:51], v[204:207]\n"
      "v_exp_f32 v200, v200\n"
      "v_cvt_pk_bf16_f32 v139, v198, v199\n"
      "s_waitcnt lgkmcnt(0)\n"
      "v_mfma_f32_16x16x32_bf16 v[192:195], a[64:67], a[0:3], 0\n"
      "ds_read_b128 a[80:83], v209 offset:8448\n"
      "v_exp_f32 v201, v201\n"
      "v_mfma_f32_16x16x32_bf16 v[192:195], a[68:71], a[4:7], v[192:195]\n"
      "v_exp_f32 v202, v202\n"
      "v_cvt_pk_bf16_f32 v146, v200, v201\n"
      "v_mfma_f32_16x16x32_bf16 v[192:195], a[72:75], a[8:11], v[192:195]\n"
      "ds_read_b128 a[84:87], v209 offset:8960\n"
      "v_exp_f32 v203, v203\n"
      "v_mfma_f32_16x16x32_bf16 v[192:195], a[76:79], a[12:15], v[192:195]\n"
      "v_exp_f32 v204, v204\n"
      "v_cvt_pk_bf16_f32 v147, v202, v203\n"
      "v_mfma_f32_16x16x32_bf16 v[196:199], a[76:79], a[28:31], 0\n"
      "ds_read_b128 a[88:91], v209 offset:9472\n"
      "v_exp_f32 v205, v205\n"
      "v_mfma_f32_16x16x32_bf16 v[196:199], a[72:75], a[24:27], v[196:199]\n"
      "v_exp_f32 v206, v206\n"
      "v_cvt_pk_bf16_f32 v154, v204, v205\n"
      "v_mfma_f32_16x16x32_bf16 v[196:199], a[68:71], a[20:23], v[196:199]\n"
      "ds_read_b128 a[92:95], v209 offset:9984\n"
      "v_exp_f32 v207, v207\n"
      "v_mfma_f32_16x16x32_bf16 v[196:199], a[64:67], a[16:19], v[196:199]\n"
      "v_exp_f32 v192, v192\n"
      "v_cvt_pk_bf16_f32 v155, v206, v207\n"
      "v_mfma_f32_16x16x32_bf16 v[200:203], a[64:67], a[32:35], 0\n"
      "v_exp_f32 v193, v193\n"
      "v_mfma_f32_16x16x32_bf16 v[200:203], a[68:71], a[36:39], v[200:203]\n"
      "v_exp_f32 v194, v194\n"
      "v_cvt_pk_bf16_f32 v132, v192, v193\n"
      "v_mfma_f32_16x16x32_bf16 v[200:203], a[72:75], a[40:43], v[200:203]\n"
      "buffer_load_dwordx4 v217, s[40:43], s80 offen offset:3072 lds\n"
      "v_exp_f32 v195, v195\n"
      "v_mfma_f32_16x16x32_bf16 v[200:203], a[76:79], a[44:47], v[200:203]\n"
      "v_exp_f32 v196, v196\n"
      "v_cvt_pk_bf16_f32 v133, v194, v195\n"
      "v_mfma_f32_16x16x32_bf16 v[204:207], a[76:79], a[60:63], 0\n"
      "ds_read_b64_tr_b16 a[160:161], v210 offset:32768\n"
      "v_exp_f32 v197, v197\n"
      "v_mfma_f32_16x16x32_bf16 v[204:207], a[72:75], a[56:59], v[204:207]\n"
      "v_exp_f32 v198, v198\n"
      "v_cvt_pk_bf16_f32 v140, v196, v197\n"
      "v_mfma_f32_16x16x32_bf16 v[204:207], a[68:71], a[52:55], v[204:207]\n"
      "ds_read_b64_tr_b16 a[162:163], v211 offset:33024\n"
      "v_exp_f32 v199, v199\n"
      "v_mfma_f32_16x16x32_bf16 v[204:207], a[64:67], a[48:51], v[204:207]\n"
      "v_exp_f32 v200, v200\n"
      "v_cvt_pk_bf16_f32 v141, v198, v199\n"
      "s_waitcnt lgkmcnt(2)\n"
      "v_mfma_f32_16x16x32_bf16 v[192:195], a[80:83], a[0:3], 0\n"
      "ds_read_b64_tr_b16 a[164:165], v210 offset:40960\n"
      "v_exp_f32 v201, v201\n"
      "v_mfma_f32_16x16x32_bf16 v[192:195], a[84:87], a[4:7], v[192:195]\n"
      "v_exp_f32 v202, v202\n"
      "v_cvt_pk_bf16_f32 v148, v200, v201\n"
      "v_mfma_f32_16x16x32_bf16 v[192:195], a[88:91], a[8:11], v[192:195]\n"
      "ds_read_b64_tr_b16 a[166:167], v211 offset:41216\n"
      "v_exp_f32 v203, v203\n"
      "v_mfma_f32_16x16x32_bf16 v[192:195], a[92:95], a[12:15], v[192:195]\n"
      "v_exp_f32 v204, v204\n"
      "v_cvt_pk_bf16_f32 v149, v202, v203\n"
      "v_mfma_f32_16x16x32_bf16 v[196:199], a[92:95], a[28:31], 0\n"
      "ds_read_b64_tr_b16 a[168:169], v212 offset:32768\n"
      "v_exp_f32 v205, v205\n"
      "v_mfma_f32_16x16x32_bf16 v[196:199], a[88:91], a[24:27], v[196:199]\n"
      "v_exp_f32 v206, v206\n"
      "v_cvt_pk_bf16_f32 v156, v204, v205\n"
      "v_mfma_f32_16x16x32_bf16 v[196:199], a[84:87], a[20:23], v[196:199]\n"
      "ds_read_b64_tr_b16 a[170:171], v213 offset:33024\n"
      "v_exp_f32 v207, v207\n"
      "v_mfma_f32_16x16x32_bf16 v[196:199], a[80:83], a[16:19], v[196:199]\n"
      "v_exp_f32 v192, v192\n"
      "v_cvt_pk_bf16_f32 v157, v206, v207\n"
      "v_mfma_f32_16x16x32_bf16 v[200:203], a[80:83], a[32:35], 0\n"
      "ds_read_b64_tr_b16 a[172:173], v212 offset:40960\n"
      "v_exp_f32 v193, v193\n"
      "v_mfma_f32_16x16x32_bf16 v[200:203], a[84:87], a[36:39], v[200:203]\n"
      "v_exp_f32 v194, v194\n"
      "v_cvt_pk_bf16_f32 v134, v192, v193\n"
      "v_mfma_f32_16x16x32_bf16 v[200:203], a[88:91], a[40:43], v[200:203]\n"
      "ds_read_b64_tr_b16 a[174:175], v213 offset:41216\n"
      "v_exp_f32 v195, v195\n"
      "v_mfma_f32_16x16x32_bf16 v[200:203], a[92:95], a[44:47], v[200:203]\n"
      "v_exp_f32 v196, v196\n"
      "v_cvt_pk_bf16_f32 v135, v194, v195\n"
      "v_mfma_f32_16x16x32_bf16 v[204:207], a[92:95], a[60:63], 0\n"
      "v_exp_f32 v197, v197\n"
      "v_mfma_f32_16x16x32_bf16 v[204:207], a[88:91], a[56:59], v[204:207]\n"
      "v_exp_f32 v198, v198\n"
      "v_cvt_pk_bf16_f32 v142, v196, v197\n"
      "v_mfma_f32_16x16x32_bf16 v[204:207], a[84:87], a[52:55], v[204:207]\n"
      "v_exp_f32 v199, v199\n"
      "v_mfma_f32_16x16x32_bf16 v[204:207], a[80:83], a[48:51], v[204:207]\n"
      "v_exp_f32 v200, v200\n"
      "v_cvt_pk_bf16_f32 v143, v198, v199\n"
      "s_waitcnt lgkmcnt(0)\n"
      "v_mfma_f32_16x16x32_bf16 v[0:3], a[96:99], v[160:163], v[0:3]\n"
      "v_exp_f32 v201, v201\n"
      "v_mfma_f32_16x16x32_bf16 v[0:3], a[100:103], v[164:167], v[0:3]\n"
      "v_exp_f32 v202, v202\n"
      "v_cvt_pk_bf16_f32 v150, v200, v201\n"
      "v_mfma_f32_16x16x32_bf16 v[0:3], a[164:167], v[132:135], v[0:3]\n"
      "v_exp_f32 v203, v203\n"
      "v_mfma_f32_16x16x32_bf16 v[0:3], a[160:163], v[128:131], v[0:3]\n"
      "v_exp_f32 v204, v204\n"
      "v_cvt_pk_bf16_f32 v151, v202, v203\n"
      "v_mfma_f32_16x16x32_bf16 v[4:7], a[104:107], v[160:163], v[4:7]\n"
      "v_exp_f32 v205, v205\n"
      "v_mfma_f32_16x16x32_bf16 v[4:7], a[108:111], v[164:167], v[4:7]\n"
      "v_exp_f32 v206, v206\n"
      "v_cvt_pk_bf16_f32 v158, v204, v205\n"
      "v_mfma_f32_16x16x32_bf16 v[4:7], a[172:175], v[132:135], v[4:7]\n"
      "v_exp_f32 v207, v207\n"
      "v_mfma_f32_16x16x32_bf16 v[4:7], a[168:171], v[128:131], v[4:7]\n"
      "v_cvt_pk_bf16_f32 v159, v206, v207\n"
      "v_mfma_f32_16x16x32_bf16 v[36:39], a[104:107], v[168:171], v[36:39]\n"
      "v_cvt_pk_bf16_f32 v220, v0, v1\n"
      "v_cvt_pk_bf16_f32 v221, v2, v3\n"
      "v_mfma_f32_16x16x32_bf16 v[36:39], a[108:111], v[172:175], v[36:39]\n"
      "ds_read_b64_tr_b16 a[114:115], v211 offset:18176\n"
      "ds_read_b64_tr_b16 a[112:113], v210 offset:17920\n"
      "v_mfma_f32_16x16x32_bf16 v[36:39], a[172:175], v[140:143], v[36:39]\n"
      "ds_read_b64_tr_b16 a[118:119], v211 offset:26368\n"
      "ds_read_b64_tr_b16 a[116:117], v210 offset:26112\n"
      "v_mfma_f32_16x16x32_bf16 v[36:39], a[168:171], v[136:139], v[36:39]\n"
      "v_cvt_pk_bf16_f32 v222, v4, v5\n"
      "v_cvt_pk_bf16_f32 v223, v6, v7\n"
      "v_mfma_f32_16x16x32_bf16 v[32:35], a[96:99], v[168:171], v[32:35]\n"
      "buffer_store_dwordx4 v[220:223], v252, s[44:47], s76 offen offset:0 sc1\n"
      "v_mfma_f32_16x16x32_bf16 v[32:35], a[100:103], v[172:175], v[32:35]\n"
      "ds_write_b128 v248, v[220:223] offset:0\n"
      "v_mfma_f32_16x16x32_bf16 v[32:35], a[164:167], v[140:143], v[32:35]\n"
      "ds_read_b64_tr_b16 a[122:123], v213 offset:18176\n"
      "ds_read_b64_tr_b16 a[120:121], v212 offset:17920\n"
      "v_mfma_f32_16x16x32_bf16 v[32:35], a[160:163], v[136:139], v[32:35]\n"
      "ds_read_b64_tr_b16 a[126:127], v213 offset:26368\n"
      "ds_read_b64_tr_b16 a[124:125], v212 offset:26112\n"
      "v_mfma_f32_16x16x32_bf16 v[64:67], a[96:99], v[176:179], v[64:67]\n"
      "v_cvt_pk_bf16_f32 v226, v36, v37\n"
      "v_cvt_pk_bf16_f32 v227, v38, v39\n"
      "v_mfma_f32_16x16x32_bf16 v[64:67], a[100:103], v[180:183], v[64:67]\n"
      "ds_read_b64_tr_b16 a[178:179], v211 offset:34560\n"
      "ds_read_b64_tr_b16 a[176:177], v210 offset:34304\n"
      "v_mfma_f32_16x16x32_bf16 v[64:67], a[164:167], v[148:151], v[64:67]\n"
      "ds_read_b64_tr_b16 a[182:183], v211 offset:42752\n"
      "ds_read_b64_tr_b16 a[180:181], v210 offset:42496\n"
      "v_mfma_f32_16x16x32_bf16 v[64:67], a[160:163], v[144:147], v[64:67]\n"
      "v_cvt_pk_bf16_f32 v224, v32, v33\n"
      "v_cvt_pk_bf16_f32 v225, v34, v35\n"
      "v_mfma_f32_16x16x32_bf16 v[68:71], a[104:107], v[176:179], v[68:71]\n"
      "buffer_store_dwordx4 v[224:227], v253, s[44:47], s77 offen offset:0 sc1\n"
      "v_mfma_f32_16x16x32_bf16 v[68:71], a[108:111], v[180:183], v[68:71]\n"
      "ds_write_b128 v249, v[224:227] offset:0\n"
      "v_mfma_f32_16x16x32_bf16 v[68:71], a[172:175], v[148:151], v[68:71]\n"
      "ds_read_b64_tr_b16 a[186:187], v213 offset:34560\n"
      "ds_read_b64_tr_b16 a[184:185], v212 offset:34304\n"
      "v_mfma_f32_16x16x32_bf16 v[68:71], a[168:171], v[144:147], v[68:71]\n"
      "ds_read_b64_tr_b16 a[190:191], v213 offset:42752\n"
      "ds_read_b64_tr_b16 a[188:189], v212 offset:42496\n"
      "v_mfma_f32_16x16x32_bf16 v[100:103], a[104:107], v[184:187], v[100:103]\n"
      "v_cvt_pk_bf16_f32 v228, v64, v65\n"
      "v_cvt_pk_bf16_f32 v229, v66, v67\n"
      "v_mfma_f32_16x16x32_bf16 v[100:103], a[108:111], v[188:191], v[100:103]\n"
      "s_lshl_b32 s74, s71, 3\n"
      "s_add_u32 s74, s74, s64\n"
      "v_mfma_f32_16x16x32_bf16 v[100:103], a[172:175], v[156:159], v[100:103]\n"
      "s_lshl_b32 s74, s74, 7\n"
      "s_add_u32 s74, s74, s67\n"
      "v_mfma_f32_16x16x32_bf16 v[100:103], a[168:171], v[152:155], v[100:103]\n"
      "v_cvt_pk_bf16_f32 v230, v68, v69\n"
      "v_cvt_pk_bf16_f32 v231, v70, v71\n"
      "v_mfma_f32_16x16x32_bf16 v[96:99], a[96:99], v[184:187], v[96:99]\n"
      "buffer_store_dwordx4 v[228:231], v254, s[44:47], s78 offen offset:0 sc1\n"
      "v_mfma_f32_16x16x32_bf16 v[96:99], a[100:103], v[188:191], v[96:99]\n"
      "ds_write_b128 v250, v[228:231] offset:0\n"
      "v_mfma_f32_16x16x32_bf16 v[96:99], a[164:167], v[156:159], v[96:99]\n"
      "v_lshlrev_b32 v236, 3, v242\n"
      "v_mov_b32 v238, 0x7fff0000\n"
      "v_mfma_f32_16x16x32_bf16 v[96:99], a[160:163], v[152:155], v[96:99]\n"
      "s_waitcnt lgkmcnt(1)\n"
      "s_mul_i32 s92, s73, 13\n"
      "v_mfma_f32_16x16x32_bf16 v[24:27], a[112:115], v[160:163], v[24:27]\n"
      "v_cvt_pk_bf16_f32 v234, v100, v101\n"
      "v_cvt_pk_bf16_f32 v235, v102, v103\n"
      "v_mfma_f32_16x16x32_bf16 v[24:27], a[116:119], v[164:167], v[24:27]\n"
      "s_lshr_b32 s93, s92, 6\n"
      "s_add_u32 s92, s92, 12\n"
      "v_mfma_f32_16x16x32_bf16 v[24:27], a[180:183], v[132:135], v[24:27]\n"
      "s_min_u32 s92, s92, 255\n"
      "s_lshr_b32 s92, s92, 6\n"
      "v_mfma_f32_16x16x32_bf16 v[24:27], a[176:179], v[128:131], v[24:27]\n"
      "v_cvt_pk_bf16_f32 v232, v96, v97\n"
      "v_cvt_pk_bf16_f32 v233, v98, v99\n"
      "v_mfma_f32_16x16x32_bf16 v[28:31], a[120:123], v[160:163], v[28:31]\n"
      "buffer_store_dwordx4 v[232:235], v255, s[44:47], s79 offen offset:0 sc1\n"
      "v_mfma_f32_16x16x32_bf16 v[28:31], a[124:127], v[164:167], v[28:31]\n"
      "ds_write_b128 v251, v[232:235] offset:0\n"
      "v_mfma_f32_16x16x32_bf16 v[28:31], a[188:191], v[132:135], v[28:31]\n"
      "s_lshl_b32 s60, 0x1010101, s93\n"
      "s_lshl_b32 s61, 0x1010101, s92\n"
      "v_mfma_f32_16x16x32_bf16 v[28:31], a[184:187], v[128:131], v[28:31]\n"
      "s_or_b32 s60, s60, s61\n"
      "s_and_b32 s61, s60, 15\n"
      "v_mfma_f32_16x16x32_bf16 v[60:63], a[120:123], v[168:171], v[60:63]\n"
      "v_cvt_pk_bf16_f32 v220, v24, v25\n"
      "v_cvt_pk_bf16_f32 v221, v26, v27\n"
      "v_mfma_f32_16x16x32_bf16 v[60:63], a[124:127], v[172:175], v[60:63]\n"
      "ds_read_b64_tr_b16 a[98:99], v211 offset:17152\n"
      "ds_read_b64_tr_b16 a[96:97], v210 offset:16896\n"
      "v_mfma_f32_16x16x32_bf16 v[60:63], a[188:191], v[140:143], v[60:63]\n"
      "ds_read_b64_tr_b16 a[102:103], v211 offset:25344\n"
      "ds_read_b64_tr_b16 a[100:101], v210 offset:25088\n"
      "v_mfma_f32_16x16x32_bf16 v[60:63], a[184:187], v[136:139], v[60:63]\n"
      "v_cvt_pk_bf16_f32 v222, v28, v29\n"
      "v_cvt_pk_bf16_f32 v223, v30, v31\n"
      "v_mfma_f32_16x16x32_bf16 v[56:59], a[112:115], v[168:171], v[56:59]\n"
      "buffer_store_dwordx4 v[220:223], v252, s[44:47], s76 offen offset:3072 sc1\n"
      "buffer_store_dword v31, v243, s[44:47], s75 offen offset:0 sc1\n"
      "v_mfma_f32_16x16x32_bf16 v[56:59], a[116:119], v[172:175], v[56:59]\n"
      "ds_write_b128 v248, v[220:223] offset:3072\n"
      "v_mfma_f32_16x16x32_bf16 v[56:59], a[180:183], v[140:143], v[56:59]\n"
      "ds_read_b64_tr_b16 a[106:107], v213 offset:17152\n"
      "ds_read_b64_tr_b16 a[104:105], v212 offset:16896\n"
      "v_mfma_f32_16x16x32_bf16 v[56:59], a[176:179], v[136:139], v[56:59]\n"
      "ds_read_b64_tr_b16 a[110:111], v213 offset:25344\n"
      "ds_read_b64_tr_b16 a[108:109], v212 offset:25088\n"
      "v_mfma_f32_16x16x32_bf16 v[88:91], a[112:115], v[176:179], v[88:91]\n"
      "v_cvt_pk_bf16_f32 v226, v60, v61\n"
      "v_cvt_pk_bf16_f32 v227, v62, v63\n"
      "v_mfma_f32_16x16x32_bf16 v[88:91], a[116:119], v[180:183], v[88:91]\n"
      "ds_read_b64_tr_b16 a[162:163], v211 offset:33536\n"
      "ds_read_b64_tr_b16 a[160:161], v210 offset:33280\n"
      "v_mfma_f32_16x16x32_bf16 v[88:91], a[180:183], v[148:151], v[88:91]\n"
      "ds_read_b64_tr_b16 a[166:167], v211 offset:41728\n"
      "ds_read_b64_tr_b16 a[164:165], v210 offset:41472\n"
      "v_mfma_f32_16x16x32_bf16 v[88:91], a[176:179], v[144:147], v[88:91]\n"
      "v_cvt_pk_bf16_f32 v224, v56, v57\n"
      "v_cvt_pk_bf16_f32 v225, v58, v59\n"
      "v_mfma_f32_16x16x32_bf16 v[92:95], a[120:123], v[176:179], v[92:95]\n"
      "buffer_store_dwordx4 v[224:227], v253, s[44:47], s77 offen offset:3072 sc1\n"
      "buffer_store_dword v63, v243, s[44:47], s75 offen offset:64 sc1\n"
      "v_mfma_f32_16x16x32_bf16 v[92:95], a[124:127], v[180:183], v[92:95]\n"
      "ds_write_b128 v249, v[224:227] offset:3072\n"
      "v_mfma_f32_16x16x32_bf16 v[92:95], a[188:191], v[148:151], v[92:95]\n"
      "ds_read_b64_tr_b16 a[170:171], v213 offset:33536\n"
      "ds_read_b64_tr_b16 a[168:169], v212 offset:33280\n"
      "v_mfma_f32_16x16x32_bf16 v[92:95], a[184:187], v[144:147], v[92:95]\n"
      "ds_read_b64_tr_b16 a[174:175], v213 offset:41728\n"
      "ds_read_b64_tr_b16 a[172:173], v212 offset:41472\n"
      "v_mfma_f32_16x16x32_bf16 v[124:127], a[120:123], v[184:187], v[124:127]\n"
      "v_cvt_pk_bf16_f32 v228, v88, v89\n"
      "v_cvt_pk_bf16_f32 v229, v90, v91\n"
      "v_mfma_f32_16x16x32_bf16 v[124:127], a[124:127], v[188:191], v[124:127]\n"
      "v_cndmask_b32 v236, v238, v236, s[60:61]\n"
      "v_lshrrev_b32 v237, 2, v246\n"
      "v_mfma_f32_16x16x32_bf16 v[124:127], a[188:191], v[156:159], v[124:127]\n"
      "v_and_b32 v237, 0x3c0, v237\n"
      "v_and_b32 v238, 0xfffff030, v246\n"
      "v_mfma_f32_16x16x32_bf16 v[124:127], a[184:187], v[152:155], v[124:127]\n"
      "v_cvt_pk_bf16_f32 v230, v92, v93\n"
      "v_cvt_pk_bf16_f32 v231, v94, v95\n"
      "v_mfma_f32_16x16x32_bf16 v[120:123], a[112:115], v[184:187], v[120:123]\n"
      "buffer_store_dwordx4 v[228:231], v254, s[44:47], s78 offen offset:3072 sc1\n"
      "buffer_store_dword v95, v243, s[44:47], s75 offen offset:128 sc1\n"
      "v_mfma_f32_16x16x32_bf16 v[120:123], a[116:119], v[188:191], v[120:123]\n"
      "ds_write_b128 v250, v[228:231] offset:3072\n"
      "v_mfma_f32_16x16x32_bf16 v[120:123], a[180:183], v[156:159], v[120:123]\n"
      "v_or_b32 v237, v237, v238\n"
      "s_mul_i32 s80, s70, 272384\n"
      "v_mfma_f32_16x16x32_bf16 v[120:123], a[176:179], v[152:155], v[120:123]\n"
      "s_waitcnt lgkmcnt(1)\n"
      "s_add_u32 s80, s80, 8388608\n"
      "v_mfma_f32_16x16x32_bf16 v[8:11], a[96:99], v[160:163], v[8:11]\n"
      "v_cvt_pk_bf16_f32 v234, v124, v125\n"
      "v_cvt_pk_bf16_f32 v235, v126, v127\n"
      "v_mfma_f32_16x16x32_bf16 v[8:11], a[100:103], v[164:167], v[8:11]\n"
      "s_lshl_b32 s87, s70, 10\n"
      "s_sub_u32 s87, s67, s87\n"
      "v_mfma_f32_16x16x32_bf16 v[8:11], a[164:167], v[132:135], v[8:11]\n"
      "s_sub_u32 s88, s87, 1024\n"
      "s_sub_u32 s87, s87, 5120\n"
      "v_mfma_f32_16x16x32_bf16 v[8:11], a[160:163], v[128:131], v[8:11]\n"
      "v_cvt_pk_bf16_f32 v232, v120, v121\n"
      "v_cvt_pk_bf16_f32 v233, v122, v123\n"
      "v_mfma_f32_16x16x32_bf16 v[12:15], a[104:107], v[160:163], v[12:15]\n"
      "buffer_store_dwordx4 v[232:235], v255, s[44:47], s79 offen offset:3072 sc1\n"
      "buffer_store_dword v127, v243, s[44:47], s75 offen offset:192 sc1\n"
      "v_mfma_f32_16x16x32_bf16 v[12:15], a[108:111], v[164:167], v[12:15]\n"
      "ds_write_b128 v251, v[232:235] offset:3072\n"
      "v_mfma_f32_16x16x32_bf16 v[12:15], a[172:175], v[132:135], v[12:15]\n"
      "s_add_u32 s81, s71, 1\n"
      "s_sub_u32 s82, s81, 5\n"
      "v_mfma_f32_16x16x32_bf16 v[12:15], a[168:171], v[128:131], v[12:15]\n"
      "s_cselect_b32 s81, s81, s82\n"
      "s_lshl_b32 s81, s81, 16\n"
      "v_mfma_f32_16x16x32_bf16 v[44:47], a[104:107], v[168:171], v[44:47]\n"
      "v_cvt_pk_bf16_f32 v220, v8, v9\n"
      "v_cvt_pk_bf16_f32 v221, v10, v11\n"
      "v_mfma_f32_16x16x32_bf16 v[44:47], a[108:111], v[172:175], v[44:47]\n"
      "ds_read_b64_tr_b16 a[114:115], v211 offset:17664\n"
      "ds_read_b64_tr_b16 a[112:113], v210 offset:17408\n"
      "v_mfma_f32_16x16x32_bf16 v[44:47], a[172:175], v[140:143], v[44:47]\n"
      "ds_read_b64_tr_b16 a[118:119], v211 offset:25856\n"
      "ds_read_b64_tr_b16 a[116:117], v210 offset:25600\n"
      "v_mfma_f32_16x16x32_bf16 v[44:47], a[168:171], v[136:139], v[44:47]\n"
      "v_cvt_pk_bf16_f32 v222, v12, v13\n"
      "v_cvt_pk_bf16_f32 v223, v14, v15\n"
      "v_mfma_f32_16x16x32_bf16 v[40:43], a[96:99], v[168:171], v[40:43]\n"
      "buffer_store_dwordx4 v[220:223], v252, s[44:47], s76 offen offset:1024 sc1\n"
      "v_mfma_f32_16x16x32_bf16 v[40:43], a[100:103], v[172:175], v[40:43]\n"
      "ds_write_b128 v248, v[220:223] offset:1024\n"
      "v_mfma_f32_16x16x32_bf16 v[40:43], a[164:167], v[140:143], v[40:43]\n"
      "ds_read_b64_tr_b16 a[122:123], v213 offset:17664\n"
      "ds_read_b64_tr_b16 a[120:121], v212 offset:17408\n"
      "v_mfma_f32_16x16x32_bf16 v[40:43], a[160:163], v[136:139], v[40:43]\n"
      "ds_read_b64_tr_b16 a[126:127], v213 offset:25856\n"
      "ds_read_b64_tr_b16 a[124:125], v212 offset:25600\n"
      "v_mfma_f32_16x16x32_bf16 v[72:75], a[96:99], v[176:179], v[72:75]\n"
      "v_cvt_pk_bf16_f32 v226, v44, v45\n"
      "v_cvt_pk_bf16_f32 v227, v46, v47\n"
      "v_mfma_f32_16x16x32_bf16 v[72:75], a[100:103], v[180:183], v[72:75]\n"
      "ds_read_b64_tr_b16 a[178:179], v211 offset:34048\n"
      "ds_read_b64_tr_b16 a[176:177], v210 offset:33792\n"
      "v_mfma_f32_16x16x32_bf16 v[72:75], a[164:167], v[148:151], v[72:75]\n"
      "ds_read_b64_tr_b16 a[182:183], v211 offset:42240\n"
      "ds_read_b64_tr_b16 a[180:181], v210 offset:41984\n"
      "v_mfma_f32_16x16x32_bf16 v[72:75], a[160:163], v[144:147], v[72:75]\n"
      "v_cvt_pk_bf16_f32 v224, v40, v41\n"
      "v_cvt_pk_bf16_f32 v225, v42, v43\n"
      "v_mfma_f32_16x16x32_bf16 v[76:79], a[104:107], v[176:179], v[76:79]\n"
      "buffer_store_dwordx4 v[224:227], v253, s[44:47], s77 offen offset:1024 sc1\n"
      "v_mfma_f32_16x16x32_bf16 v[76:79], a[108:111], v[180:183], v[76:79]\n"
      "ds_write_b128 v249, v[224:227] offset:1024\n"
      "v_mfma_f32_16x16x32_bf16 v[76:79], a[172:175], v[148:151], v[76:79]\n"
      "ds_read_b64_tr_b16 a[186:187], v213 offset:34048\n"
      "ds_read_b64_tr_b16 a[184:185], v212 offset:33792\n"
      "v_mfma_f32_16x16x32_bf16 v[76:79], a[168:171], v[144:147], v[76:79]\n"
      "ds_read_b64_tr_b16 a[190:191], v213 offset:42240\n"
      "ds_read_b64_tr_b16 a[188:189], v212 offset:41984\n"
      "v_mfma_f32_16x16x32_bf16 v[108:111], a[104:107], v[184:187], v[108:111]\n"
      "v_cvt_pk_bf16_f32 v228, v72, v73\n"
      "v_cvt_pk_bf16_f32 v229, v74, v75\n"
      "v_mfma_f32_16x16x32_bf16 v[108:111], a[108:111], v[188:191], v[108:111]\n"
      "s_add_u32 s82, s80, s81\n"
      "s_add_u32 s81, s71, 2\n"
      "v_mfma_f32_16x16x32_bf16 v[108:111], a[172:175], v[156:159], v[108:111]\n"
      "s_sub_u32 s83, s81, 5\n"
      "s_cselect_b32 s81, s81, s83\n"
      "v_mfma_f32_16x16x32_bf16 v[108:111], a[168:171], v[152:155], v[108:111]\n"
      "v_cvt_pk_bf16_f32 v230, v76, v77\n"
      "v_cvt_pk_bf16_f32 v231, v78, v79\n"
      "v_mfma_f32_16x16x32_bf16 v[104:107], a[96:99], v[184:187], v[104:107]\n"
      "buffer_store_dwordx4 v[228:231], v254, s[44:47], s78 offen offset:1024 sc1\n"
      "v_mfma_f32_16x16x32_bf16 v[104:107], a[100:103], v[188:191], v[104:107]\n"
      "ds_write_b128 v250, v[228:231] offset:1024\n"
      "v_mfma_f32_16x16x32_bf16 v[104:107], a[164:167], v[156:159], v[104:107]\n"
      "s_lshl_b32 s81, s81, 16\n"
      "s_add_u32 s83, s80, s81\n"
      "v_mfma_f32_16x16x32_bf16 v[104:107], a[160:163], v[152:155], v[104:107]\n"
      "s_waitcnt lgkmcnt(1)\n"
      "s_add_u32 s81, s71, 3\n"
      "v_mfma_f32_16x16x32_bf16 v[16:19], a[112:115], v[160:163], v[16:19]\n"
      "v_cvt_pk_bf16_f32 v234, v108, v109\n"
      "v_cvt_pk_bf16_f32 v235, v110, v111\n"
      "v_mfma_f32_16x16x32_bf16 v[16:19], a[116:119], v[164:167], v[16:19]\n"
      "s_sub_u32 s84, s81, 5\n"
      "s_cselect_b32 s81, s81, s84\n"
      "v_mfma_f32_16x16x32_bf16 v[16:19], a[180:183], v[132:135], v[16:19]\n"
      "s_lshl_b32 s81, s81, 16\n"
      "s_add_u32 s84, s80, s81\n"
      "v_mfma_f32_16x16x32_bf16 v[16:19], a[176:179], v[128:131], v[16:19]\n"
      "v_cvt_pk_bf16_f32 v232, v104, v105\n"
      "v_cvt_pk_bf16_f32 v233, v106, v107\n"
      "v_mfma_f32_16x16x32_bf16 v[20:23], a[120:123], v[160:163], v[20:23]\n"
      "buffer_store_dwordx4 v[232:235], v255, s[44:47], s79 offen offset:1024 sc1\n"
      "v_mfma_f32_16x16x32_bf16 v[20:23], a[124:127], v[164:167], v[20:23]\n"
      "ds_write_b128 v251, v[232:235] offset:1024\n"
      "v_mfma_f32_16x16x32_bf16 v[20:23], a[188:191], v[132:135], v[20:23]\n"
      "s_waitcnt vmcnt(4)\n"
      "buffer_store_dword v214, v243, s[44:47], s74 offen offset:0 sc1\n"
      "v_mfma_f32_16x16x32_bf16 v[20:23], a[184:187], v[128:131], v[20:23]\n"
      "s_add_u32 s81, s71, 4\n"
      "s_sub_u32 s85, s81, 5\n"
      "v_mfma_f32_16x16x32_bf16 v[52:55], a[120:123], v[168:171], v[52:55]\n"
      "v_cvt_pk_bf16_f32 v220, v16, v17\n"
      "v_cvt_pk_bf16_f32 v221, v18, v19\n"
      "v_mfma_f32_16x16x32_bf16 v[52:55], a[124:127], v[172:175], v[52:55]\n"
      "ds_read_b128 a[64:67], v208 offset:16384\n"
      "s_cselect_b32 s81, s81, s85\n"
      "v_mfma_f32_16x16x32_bf16 v[52:55], a[188:191], v[140:143], v[52:55]\n"
      "ds_read_b128 a[68:71], v208 offset:16896\n"
      "s_lshl_b32 s81, s81, 16\n"
      "v_mfma_f32_16x16x32_bf16 v[52:55], a[184:187], v[136:139], v[52:55]\n"
      "v_cvt_pk_bf16_f32 v222, v20, v21\n"
      "v_cvt_pk_bf16_f32 v223, v22, v23\n"
      "v_mfma_f32_16x16x32_bf16 v[48:51], a[112:115], v[168:171], v[48:51]\n"
      "buffer_store_dwordx4 v[220:223], v252, s[44:47], s76 offen offset:2048 sc1\n"
      "v_mfma_f32_16x16x32_bf16 v[48:51], a[116:119], v[172:175], v[48:51]\n"
      "ds_write_b128 v248, v[220:223] offset:2048\n"
      "v_mfma_f32_16x16x32_bf16 v[48:51], a[180:183], v[140:143], v[48:51]\n"
      "ds_read_b128 a[72:75], v208 offset:17408\n"
      "s_add_u32 s85, s80, s81\n"
      "v_mfma_f32_16x16x32_bf16 v[48:51], a[176:179], v[136:139], v[48:51]\n"
      "ds_read_b128 a[76:79], v208 offset:17920\n"
      "v_mfma_f32_16x16x32_bf16 v[80:83], a[112:115], v[176:179], v[80:83]\n"
      "v_cvt_pk_bf16_f32 v226, v52, v53\n"
      "v_cvt_pk_bf16_f32 v227, v54, v55\n"
      "v_mfma_f32_16x16x32_bf16 v[80:83], a[116:119], v[180:183], v[80:83]\n"
      "ds_read_b128 a[80:83], v209 offset:16640\n"
      "v_mfma_f32_16x16x32_bf16 v[80:83], a[180:183], v[148:151], v[80:83]\n"
      "ds_read_b128 a[84:87], v209 offset:17152\n"
      "v_mfma_f32_16x16x32_bf16 v[80:83], a[176:179], v[144:147], v[80:83]\n"
      "v_cvt_pk_bf16_f32 v224, v48, v49\n"
      "v_cvt_pk_bf16_f32 v225, v50, v51\n"
      "v_mfma_f32_16x16x32_bf16 v[84:87], a[120:123], v[176:179], v[84:87]\n"
      "buffer_store_dwordx4 v[224:227], v253, s[44:47], s77 offen offset:2048 sc1\n"
      "v_mfma_f32_16x16x32_bf16 v[84:87], a[124:127], v[180:183], v[84:87]\n"
      "ds_write_b128 v249, v[224:227] offset:2048\n"
      "v_mfma_f32_16x16x32_bf16 v[84:87], a[188:191], v[148:151], v[84:87]\n"
      "ds_read_b128 a[88:91], v209 offset:17664\n"
      "v_mfma_f32_16x16x32_bf16 v[84:87], a[184:187], v[144:147], v[84:87]\n"
      "ds_read_b128 a[92:95], v209 offset:18176\n"
      "v_mfma_f32_16x16x32_bf16 v[116:119], a[120:123], v[184:187], v[116:119]\n"
      "v_cvt_pk_bf16_f32 v228, v80, v81\n"
      "v_cvt_pk_bf16_f32 v229, v82, v83\n"
      "v_mfma_f32_16x16x32_bf16 v[116:119], a[124:127], v[188:191], v[116:119]\n"
      "v_mfma_f32_16x16x32_bf16 v[116:119], a[188:191], v[156:159], v[116:119]\n"
      "v_mfma_f32_16x16x32_bf16 v[116:119], a[184:187], v[152:155], v[116:119]\n"
      "s_nop 0\n"
      "v_cvt_pk_bf16_f32 v230, v84, v85\n"
      "v_cvt_pk_bf16_f32 v231, v86, v87\n"
      "v_mfma_f32_16x16x32_bf16 v[112:115], a[112:115], v[184:187], v[112:115]\n"
      "buffer_store_dwordx4 v[228:231], v254, s[44:47], s78 offen offset:2048 sc1\n"
      "v_mfma_f32_16x16x32_bf16 v[112:115], a[116:119], v[188:191], v[112:115]\n"
      "ds_write_b128 v250, v[228:231] offset:2048\n"
      "v_mfma_f32_16x16x32_bf16 v[112:115], a[180:183], v[156:159], v[112:115]\n"
      "v_mfma_f32_16x16x32_bf16 v[112:115], a[176:179], v[152:155], v[112:115]\n"
      "s_waitcnt lgkmcnt(1)\n"
      "v_mfma_f32_16x16x32_bf16 v[144:147], a[64:67], a[192:195], 0\n"
      "v_cvt_pk_bf16_f32 v234, v116, v117\n"
      "v_cvt_pk_bf16_f32 v235, v118, v119\n"
      "v_mfma_f32_16x16x32_bf16 v[144:147], a[68:71], a[196:199], v[144:147]\n"
      "v_mfma_f32_16x16x32_bf16 v[144:147], a[72:75], a[200:203], v[144:147]\n"
      "v_mfma_f32_16x16x32_bf16 v[144:147], a[76:79], a[204:207], v[144:147]\n"
      "s_nop 0\n"
      "v_cvt_pk_bf16_f32 v232, v112, v113\n"
      "v_cvt_pk_bf16_f32 v233, v114, v115\n"
      "v_mfma_f32_16x16x32_bf16 v[148:151], a[80:83], a[192:195], 0\n"
      "buffer_store_dwordx4 v[232:235], v255, s[44:47], s79 offen offset:2048 sc1\n"
      "v_mfma_f32_16x16x32_bf16 v[148:151], a[84:87], a[196:199], v[148:151]\n"
      "ds_write_b128 v251, v[232:235] offset:2048\n"
      "v_mfma_f32_16x16x32_bf16 v[148:151], a[88:91], a[200:203], v[148:151]\n"
      "buffer_load_dword v239, v236, s[44:47], s67 offen offset:0 sc1\n"
      "v_mfma_f32_16x16x32_bf16 v[148:151], a[92:95], a[204:207], v[148:151]\n"
      "ds_read_b64_tr_b16 a[160:161], v210 offset:49152\n"
      "ds_read_b64_tr_b16 a[162:163], v211 offset:49408\n"
      "ds_read_b64_tr_b16 a[164:165], v212 offset:49152\n"
      "ds_read_b64_tr_b16 a[166:167], v213 offset:49408\n"
      "ds_read_b64_tr_b16 a[168:169], v210 offset:49664\n"
      "ds_read_b64_tr_b16 a[170:171], v211 offset:49920\n"
      "ds_read_b64_tr_b16 a[172:173], v212 offset:49664\n"
      "ds_read_b64_tr_b16 a[174:175], v213 offset:49920\n"
      "ds_read_b64_tr_b16 a[176:177], v210 offset:50176\n"
      "ds_read_b64_tr_b16 a[178:179], v211 offset:50432\n"
      "ds_read_b64_tr_b16 a[180:181], v212 offset:50176\n"
      "ds_read_b64_tr_b16 a[182:183], v213 offset:50432\n"
      "ds_read_b64_tr_b16 a[184:185], v210 offset:50688\n"
      "ds_read_b64_tr_b16 a[186:187], v211 offset:50944\n"
      "ds_read_b64_tr_b16 a[188:189], v212 offset:50688\n"
      "ds_read_b64_tr_b16 a[190:191], v213 offset:50944\n"
      "v_add_u32 v182, 98304, v242\n"
      "v_add_u32 v173, s90, v242\n"
      "v_exp_f32 v144, v144\n"
      "v_exp_f32 v145, v145\n"
      "v_exp_f32 v146, v146\n"
      "v_exp_f32 v147, v147\n"
      "v_exp_f32 v148, v148\n"
      "v_exp_f32 v149, v149\n"
      "v_exp_f32 v150, v150\n"
      "v_exp_f32 v151, v151\n"
      "v_accvgpr_read_b32 v64, a128\n"
      "v_accvgpr_read_b32 v65, a129\n"
      "v_cvt_pk_bf16_f32 v152, v144, v145\n"
      "v_cvt_pk_bf16_f32 v153, v146, v147\n"
      "v_cvt_pk_bf16_f32 v154, v148, v149\n"
      "v_cvt_pk_bf16_f32 v155, v150, v151\n"
      "s_waitcnt lgkmcnt(0)\n"
      "s_barrier\n"
      "v_mfma_f32_16x16x32_bf16 v[0:3], a[160:163], v[152:155], 0\n"
      "v_accvgpr_read_b32 v66, a130\n"
      "ds_read_b128 a[0:3], v242 offset:32768\n"
      "v_accvgpr_read_b32 v67, a131\n"
      "v_mfma_f32_16x16x32_bf16 v[4:7], a[164:167], v[152:155], 0\n"
      "v_accvgpr_read_b32 v68, a132\n"
      "ds_read_b128 a[16:19], v242 offset:36864\n"
      "v_accvgpr_read_b32 v69, a133\n"
      "v_mfma_f32_16x16x32_bf16 v[8:11], a[168:171], v[152:155], 0\n"
      "v_accvgpr_read_b32 v70, a134\n"
      "ds_read_b128 a[32:35], v242 offset:40960\n"
      "v_accvgpr_read_b32 v71, a135\n"
      "v_mfma_f32_16x16x32_bf16 v[12:15], a[172:175], v[152:155], 0\n"
      "v_accvgpr_read_b32 v72, a136\n"
      "ds_read_b128 a[48:51], v242 offset:45056\n"
      "v_accvgpr_read_b32 v73, a137\n"
      "v_mfma_f32_16x16x32_bf16 v[16:19], a[176:179], v[152:155], 0\n"
      "v_accvgpr_read_b32 v74, a138\n"
      "ds_read_b128 a[64:67], v182 offset:49152\n"
      "v_accvgpr_read_b32 v75, a139\n"
      "v_mfma_f32_16x16x32_bf16 v[20:23], a[180:183], v[152:155], 0\n"
      "v_accvgpr_read_b32 v76, a140\n"
      "ds_read_b128 a[80:83], v182 offset:53248\n"
      "v_accvgpr_read_b32 v77, a141\n"
      "v_mfma_f32_16x16x32_bf16 v[24:27], a[184:187], v[152:155], 0\n"
      "v_accvgpr_read_b32 v78, a142\n"
      "ds_read_b128 a[96:99], v182 offset:57344\n"
      "v_accvgpr_read_b32 v79, a143\n"
      "v_mfma_f32_16x16x32_bf16 v[28:31], a[188:191], v[152:155], 0\n"
      "v_accvgpr_read_b32 v80, a144\n"
      "ds_read_b128 a[112:115], v182 offset:61440\n"
      "v_accvgpr_read_b32 v81, a145\n"
      "v_accvgpr_read_b32 v82, a146\n"
      "v_accvgpr_read_b32 v83, a147\n"
      "ds_read_b128 a[4:7], v242 offset:33792\n"
      "v_accvgpr_read_b32 v84, a148\n"
      "v_accvgpr_read_b32 v85, a149\n"
      "v_accvgpr_read_b32 v86, a150\n"
      "ds_read_b128 a[20:23], v242 offset:37888\n"
      "v_accvgpr_read_b32 v87, a151\n"
      "v_accvgpr_read_b32 v88, a152\n"
      "v_accvgpr_read_b32 v89, a153\n"
      "ds_read_b128 a[36:39], v242 offset:41984\n"
      "v_accvgpr_read_b32 v90, a154\n"
      "v_accvgpr_read_b32 v91, a155\n"
      "v_accvgpr_read_b32 v92, a156\n"
      "ds_read_b128 a[52:55], v242 offset:46080\n"
      "v_accvgpr_read_b32 v93, a157\n"
      "v_accvgpr_read_b32 v94, a158\n"
      "v_accvgpr_read_b32 v95, a159\n"
      "ds_read_b128 a[68:71], v182 offset:50176\n"
      "s_waitcnt vmcnt(1)\n"
      "buffer_store_dword v214, v243, s[44:47], s74 offen offset:512 sc1\n"
      "s_waitcnt vmcnt(1)\n"
      "v_cmp_ne_u32 vcc, 0, v239\n"
      "s_nop 0\n"
      "s_andn2_b64 s[92:93], s[60:61], vcc\n"
      "s_cbranch_scc0 5f\n"
      "s_mov_b32 s80, 0x400000\n"
      "4:\n"
      "buffer_load_dword v239, v236, s[44:47], s67 offen offset:0 sc1\n"
      "s_waitcnt vmcnt(0)\n"
      "v_cmp_ne_u32 vcc, 0, v239\n"
      "s_nop 1\n"
      "s_andn2_b64 s[92:93], s[60:61], vcc\n"
      "s_cbranch_scc0 5f\n"
      "s_sub_u32 s80, s80, 1\n"
      "s_cbranch_scc0 4b\n"
      "5:\n"
      "buffer_load_dword v176, v244, s[44:47], s87 offen offset:0 sc1\n"
      "buffer_load_dword v177, v244, s[44:47], s87 offen offset:1024 sc1\n"
      "buffer_load_dword v178, v244, s[44:47], s87 offen offset:2048 sc1\n"
      "buffer_load_dword v179, v244, s[44:47], s87 offen offset:3072 sc1\n"
      "buffer_load_dword v180, v244, s[44:47], s88 offen offset:0 sc1\n"
      "buffer_load_dwordx4 v[104:107], v237, s[44:47], s82 offen offset:0 sc1\n"
      "buffer_load_dwordx4 v[108:111], v237, s[44:47], s83 offen offset:0 sc1\n"
      "buffer_load_dwordx4 v[112:115], v237, s[44:47], s84 offen offset:0 sc1\n"
      "buffer_load_dwordx4 v[116:119], v237, s[44:47], s85 offen offset:0 sc1\n"
      "buffer_load_dwordx4 v[124:127], v237, s[44:47], s82 offen offset:3072 sc1\n"
      "buffer_load_dwordx4 v[128:131], v237, s[44:47], s83 offen offset:3072 sc1\n"
      "buffer_load_dwordx4 v[132:135], v237, s[44:47], s84 offen offset:3072 sc1\n"
      "buffer_load_dwordx4 v[136:139], v237, s[44:47], s85 offen offset:3072 sc1\n"
      "v_bfe_u32 v168, v242, 6, 2\n"
      "v_lshrrev_b32 v169, 8, v242\n"
      "v_cmp_eq_u32 vcc, v168, v169\n"
      "ds_read_b128 a[84:87], v182 offset:54272\n"
      "v_and_b32 v168, 16, v242\n"
      "v_mov_b32 v169, 0x3f80\n"
      "v_lshlrev_b32 v168, v168, v169\n"
      "ds_read_b128 a[100:103], v182 offset:58368\n"
      "v_cndmask_b32 v168, 0, v168, vcc\n"
      "v_and_b32 v169, 32, v242\n"
      "v_cmp_eq_u32 vcc, 0, v169\n"
      "ds_read_b128 a[116:119], v182 offset:62464\n"
      "v_mov_b32 v160, 0\n"
      "v_mov_b32 v161, 0\n"
      "v_cndmask_b32 v162, 0, v168, vcc\n"
      "ds_read_b128 a[8:11], v242 offset:34816\n"
      "v_cndmask_b32_e64 v163, v168, 0, vcc\n"
      "v_mov_b32 v164, 0\n"
      "v_mov_b32 v165, 0\n"
      "ds_read_b128 a[24:27], v242 offset:38912\n"
      "v_accvgpr_read_b32 v170, a224\n"
      "v_lshlrev_b32 v170, 16, v170\n"
      "v_mul_f32 v170, 0.5, v170\n"
      "ds_read_b128 a[40:43], v242 offset:43008\n"
      "v_pk_mul_f32 v[64:65], v[64:65], v[170:171] op_sel_hi:[1,0]\n"
      "v_pk_mul_f32 v[66:67], v[66:67], v[170:171] op_sel_hi:[1,0]\n"
      "v_pk_mul_f32 v[68:69], v[68:69], v[170:171] op_sel_hi:[1,0]\n"
      "ds_read_b128 a[56:59], v242 offset:47104\n"
      "v_pk_mul_f32 v[70:71], v[70:71], v[170:171] op_sel_hi:[1,0]\n"
      "v_pk_mul_f32 v[72:73], v[72:73], v[170:171] op_sel_hi:[1,0]\n"
      "v_pk_mul_f32 v[74:75], v[74:75], v[170:171] op_sel_hi:[1,0]\n"
      "ds_read_b128 a[72:75], v182 offset:51200\n"
      "v_pk_mul_f32 v[76:77], v[76:77], v[170:171] op_sel_hi:[1,0]\n"
      "v_pk_mul_f32 v[78:79], v[78:79], v[170:171] op_sel_hi:[1,0]\n"
      "v_pk_mul_f32 v[80:81], v[80:81], v[170:171] op_sel_hi:[1,0]\n"
      "ds_read_b128 a[88:91], v182 offset:55296\n"
      "v_pk_mul_f32 v[82:83], v[82:83], v[170:171] op_sel_hi:[1,0]\n"
      "v_pk_mul_f32 v[84:85], v[84:85], v[170:171] op_sel_hi:[1,0]\n"
      "v_pk_mul_f32 v[86:87], v[86:87], v[170:171] op_sel_hi:[1,0]\n"
      "ds_read_b128 a[104:107], v182 offset:59392\n"
      "v_pk_mul_f32 v[88:89], v[88:89], v[170:171] op_sel_hi:[1,0]\n"
      "v_pk_mul_f32 v[90:91], v[90:91], v[170:171] op_sel_hi:[1,0]\n"
      "v_pk_mul_f32 v[92:93], v[92:93], v[170:171] op_sel_hi:[1,0]\n"
      "ds_read_b128 a[120:123], v182 offset:63488\n"
      "v_pk_mul_f32 v[94:95], v[94:95], v[170:171] op_sel_hi:[1,0]\n"
      "v_mfma_f32_16x16x32_bf16 v[32:35], v[162:165], a[208:211], 0\n"
      "v_cvt_pk_bf16_f32 v144, v64, v65\n"
      "ds_read_b128 a[12:15], v242 offset:35840\n"
      "v_cvt_pk_bf16_f32 v145, v66, v67\n"
      "v_mfma_f32_16x16x32_bf16 v[36:39], v[160:163], a[208:211], 0\n"
      "v_cvt_pk_bf16_f32 v146, v68, v69\n"
      "ds_read_b128 a[28:31], v242 offset:39936\n"
      "v_cvt_pk_bf16_f32 v147, v70, v71\n"
      "v_mfma_f32_16x16x32_bf16 v[40:43], v[162:165], a[212:215], 0\n"
      "v_cvt_pk_bf16_f32 v148, v72, v73\n"
      "ds_read_b128 a[44:47], v242 offset:44032\n"
      "v_cvt_pk_bf16_f32 v149, v74, v75\n"
      "v_mfma_f32_16x16x32_bf16 v[44:47], v[160:163], a[212:215], 0\n"
      "v_cvt_pk_bf16_f32 v150, v76, v77\n"
      "ds_read_b128 a[60:63], v242 offset:48128\n"
      "v_cvt_pk_bf16_f32 v151, v78, v79\n"
      "v_mfma_f32_16x16x32_bf16 v[48:51], v[162:165], a[216:219], 0\n"
      "v_cvt_pk_bf16_f32 v152, v80, v81\n"
      "ds_read_b128 a[76:79], v182 offset:52224\n"
      "v_cvt_pk_bf16_f32 v153, v82, v83\n"
      "v_mfma_f32_16x16x32_bf16 v[52:55], v[160:163], a[216:219], 0\n"
      "v_cvt_pk_bf16_f32 v154, v84, v85\n"
      "ds_read_b128 a[92:95], v182 offset:56320\n"
      "v_cvt_pk_bf16_f32 v155, v86, v87\n"
      "v_mfma_f32_16x16x32_bf16 v[56:59], v[162:165], a[220:223], 0\n"
      "v_cvt_pk_bf16_f32 v156, v88, v89\n"
      "ds_read_b128 a[108:111], v182 offset:60416\n"
      "v_cvt_pk_bf16_f32 v157, v90, v91\n"
      "v_mfma_f32_16x16x32_bf16 v[60:63], v[160:163], a[220:223], 0\n"
      "v_cvt_pk_bf16_f32 v158, v92, v93\n"
      "ds_read_b128 a[124:127], v182 offset:64512\n"
      "v_cvt_pk_bf16_f32 v159, v94, v95\n"
      "s_mov_b32 vcc_lo, 0\n"
      "s_mov_b32 vcc_hi, 0xffff0000\n"
      "v_cndmask_b32 v168, 0, v31, vcc\n"
      "v_mov_b32 v169, v168\n"
      "s_nop 1\n"
      "v_permlane16_swap_b32 v168, v169\n"
      "v_add_f32 v168, v168, v169\n"
      "v_mov_b32 v169, v168\n"
      "s_nop 1\n"
      "v_permlane32_swap_b32 v168, v169\n"
      "v_add_f32 v168, v168, v169\n"
      "buffer_load_dword v239, v236, s[44:47], s67 offen offset:512 sc1\n"
      "s_waitcnt lgkmcnt(1)\n"
      "v_mfma_f32_16x16x32_bf16 v[32:35], a[0:3], v[144:147], v[32:35]\n"
      "ds_read_b128 v[120:123], v173 offset:0\n"
      "ds_read_b128 v[80:83], v173 offset:1024\n"
      "v_mfma_f32_16x16x32_bf16 v[36:39], a[16:19], v[144:147], v[36:39]\n"
      "ds_read_b128 v[100:103], v173 offset:2048\n"
      "ds_read_b128 v[140:143], v173 offset:3072\n"
      "v_mfma_f32_16x16x32_bf16 v[40:43], a[32:35], v[144:147], v[40:43]\n"
      "ds_read_b128 a[0:3], v242 offset:0\n"
      "v_mfma_f32_16x16x32_bf16 v[44:47], a[48:51], v[144:147], v[44:47]\n"
      "ds_read_b128 a[16:19], v242 offset:4096\n"
      "v_mfma_f32_16x16x32_bf16 v[48:51], a[64:67], v[144:147], v[48:51]\n"
      "ds_read_b128 a[32:35], v242 offset:8192\n"
      "v_mfma_f32_16x16x32_bf16 v[52:55], a[80:83], v[144:147], v[52:55]\n"
      "ds_read_b128 a[48:51], v242 offset:12288\n"
      "v_mfma_f32_16x16x32_bf16 v[56:59], a[96:99], v[144:147], v[56:59]\n"
      "ds_read_b128 a[64:67], v242 offset:16384\n"
      "v_mfma_f32_16x16x32_bf16 v[60:63], a[112:115], v[144:147], v[60:63]\n"
      "ds_read_b128 a[80:83], v242 offset:20480\n"
      "v_mfma_f32_16x16x32_bf16 v[32:35], a[4:7], v[148:151], v[32:35]\n"
      "ds_read_b128 a[96:99], v242 offset:24576\n"
      "v_mfma_f32_16x16x32_bf16 v[36:39], a[20:23], v[148:151], v[36:39]\n"
      "ds_read_b128 a[112:115], v242 offset:28672\n"
      "v_mfma_f32_16x16x32_bf16 v[40:43], a[36:39], v[148:151], v[40:43]\n"
      "ds_read_b128 a[4:7], v242 offset:1024\n"
      "v_mfma_f32_16x16x32_bf16 v[44:47], a[52:55], v[148:151], v[44:47]\n"
      "ds_read_b128 a[20:23], v242 offset:5120\n"
      "v_mfma_f32_16x16x32_bf16 v[48:51], a[68:71], v[148:151], v[48:51]\n"
      "ds_read_b128 a[36:39], v242 offset:9216\n"
      "v_mfma_f32_16x16x32_bf16 v[52:55], a[84:87], v[148:151], v[52:55]\n"
      "ds_read_b128 a[52:55], v242 offset:13312\n"
      "v_mfma_f32_16x16x32_bf16 v[56:59], a[100:103], v[148:151], v[56:59]\n"
      "ds_read_b128 a[68:71], v242 offset:17408\n"
      "v_mfma_f32_16x16x32_bf16 v[60:63], a[116:119], v[148:151], v[60:63]\n"
      "ds_read_b128 a[84:87], v242 offset:21504\n"
      "v_mfma_f32_16x16x32_bf16 v[32:35], a[8:11], v[152:155], v[32:35]\n"
      "ds_read_b128 a[100:103], v242 offset:25600\n"
      "v_mfma_f32_16x16x32_bf16 v[36:39], a[24:27], v[152:155], v[36:39]\n"
      "ds_read_b128 a[116:119], v242 offset:29696\n"
      "v_mfma_f32_16x16x32_bf16 v[40:43], a[40:43], v[152:155], v[40:43]\n"
      "ds_read_b128 a[8:11], v242 offset:2048\n"
      "v_mfma_f32_16x16x32_bf16 v[44:47], a[56:59], v[152:155], v[44:47]\n"
      "ds_read_b128 a[24:27], v242 offset:6144\n"
      "v_mfma_f32_16x16x32_bf16 v[48:51], a[72:75], v[152:155], v[48:51]\n"
      "ds_read_b128 a[40:43], v242 offset:10240\n"
      "v_mfma_f32_16x16x32_bf16 v[52:55], a[88:91], v[152:155], v[52:55]\n"
      "ds_read_b128 a[56:59], v242 offset:14336\n"
      "v_mfma_f32_16x16x32_bf16 v[56:59], a[104:107], v[152:155], v[56:59]\n"
      "ds_read_b128 a[72:75], v242 offset:18432\n"
      "v_mfma_f32_16x16x32_bf16 v[60:63], a[120:123], v[152:155], v[60:63]\n"
      "ds_read_b128 a[88:91], v242 offset:22528\n"
      "v_mfma_f32_16x16x32_bf16 v[32:35], a[12:15], v[156:159], v[32:35]\n"
      "ds_read_b128 a[104:107], v242 offset:26624\n"
      "v_mfma_f32_16x16x32_bf16 v[36:39], a[28:31], v[156:159], v[36:39]\n"
      "ds_read_b128 a[120:123], v242 offset:30720\n"
      "v_mfma_f32_16x16x32_bf16 v[40:43], a[44:47], v[156:159], v[40:43]\n"
      "ds_read_b128 a[12:15], v242 offset:3072\n"
      "v_mfma_f32_16x16x32_bf16 v[44:47], a[60:63], v[156:159], v[44:47]\n"
      "ds_read_b128 a[28:31], v242 offset:7168\n"
      "v_mfma_f32_16x16x32_bf16 v[48:51], a[76:79], v[156:159], v[48:51]\n"
      "ds_read_b128 a[44:47], v242 offset:11264\n"
      "v_mfma_f32_16x16x32_bf16 v[52:55], a[92:95], v[156:159], v[52:55]\n"
      "ds_read_b128 a[60:63], v242 offset:15360\n"
      "v_mfma_f32_16x16x32_bf16 v[56:59], a[108:111], v[156:159], v[56:59]\n"
      "ds_read_b128 a[76:79], v242 offset:19456\n"
      "s_waitcnt lgkmcnt(9)\n"
      "v_mfma_f32_16x16x32_bf16 v[60:63], a[124:127], v[156:159], v[60:63]\n"
      "ds_read_b128 a[92:95], v242 offset:23552\n"
      "v_mfma_f32_16x16x32_bf16 v[0:3], v[162:165], v[120:123], v[0:3]\n"
      "ds_read_b128 a[108:111], v242 offset:27648\n"
      "v_mfma_f32_16x16x32_bf16 v[4:7], v[160:163], v[120:123], v[4:7]\n"
      "ds_read_b128 a[124:127], v242 offset:31744\n"
      "v_mfma_f32_16x16x32_bf16 v[8:11], v[162:165], v[80:83], v[8:11]\n"
      "v_mfma_f32_16x16x32_bf16 v[12:15], v[160:163], v[80:83], v[12:15]\n"
      "v_mfma_f32_16x16x32_bf16 v[16:19], v[162:165], v[100:103], v[16:19]\n"
      "v_mfma_f32_16x16x32_bf16 v[20:23], v[160:163], v[100:103], v[20:23]\n"
      "v_mfma_f32_16x16x32_bf16 v[24:27], v[162:165], v[140:143], v[24:27]\n"
      "v_mfma_f32_16x16x32_bf16 v[28:31], v[160:163], v[140:143], v[28:31]\n"
      "s_waitcnt vmcnt(0)\n"
      "v_cmp_ne_u32 vcc, 0, v239\n"
      "s_nop 0\n"
      "s_andn2_b64 s[92:93], s[60:61], vcc\n"
      "s_cbranch_scc0 6f\n"
      "s_mov_b32 s80, 0x400000\n"
      "3:\n"
      "buffer_load_dword v239, v236, s[44:47], s67 offen offset:512 sc1\n"
      "s_waitcnt vmcnt(0)\n"
      "v_cmp_ne_u32 vcc, 0, v239\n"
      "s_nop 1\n"
      "s_andn2_b64 s[92:93], s[60:61], vcc\n"
      "s_cbranch_scc0 6f\n"
      "s_sub_u32 s80, s80, 1\n"
      "s_cbranch_scc0 3b\n"
      "6:\n"
      "buffer_load_dwordx4 v[64:67], v237, s[44:47], s82 offen offset:1024 sc1\n"
      "buffer_load_dwordx4 v[68:71], v237, s[44:47], s83 offen offset:1024 sc1\n"
      "buffer_load_dwordx4 v[72:75], v237, s[44:47], s84 offen offset:1024 sc1\n"
      "buffer_load_dwordx4 v[76:79], v237, s[44:47], s85 offen offset:1024 sc1\n"
      "buffer_load_dwordx4 v[84:87], v237, s[44:47], s82 offen offset:2048 sc1\n"
      "buffer_load_dwordx4 v[88:91], v237, s[44:47], s83 offen offset:2048 sc1\n"
      "buffer_load_dwordx4 v[92:95], v237, s[44:47], s84 offen offset:2048 sc1\n"
      "buffer_load_dwordx4 v[96:99], v237, s[44:47], s85 offen offset:2048 sc1\n"
      "v_add_f32 v168, v168, v176\n"
      "v_add_f32 v177, v177, v178\n"
      "v_add_f32 v179, v179, v180\n"
      "v_add_f32 v168, v168, v177\n"
      "v_add_f32 v168, v168, v179\n"
      "v_cmp_le_f32 vcc, 0x21800000, v168\n"
      "s_nop 0\n"
      "s_andn2_b64 s[92:93], s[68:69], vcc\n"
      "v_cmp_ge_f32 vcc, 0x7f61b1e6, v168\n"
      "v_rcp_f32 v168, v168\n"
      "s_andn2_b64 vcc, s[68:69], vcc\n"
      "s_or_b64 vcc, vcc, s[92:93]\n"
      "s_cbranch_scc1 7f\n"
      "v_mfma_f32_16x16x32_bf16 v[0:3], v[162:165], v[104:107], v[0:3]\n"
      "v_mfma_f32_16x16x32_bf16 v[4:7], v[160:163], v[104:107], v[4:7]\n"
      "v_mfma_f32_16x16x32_bf16 v[0:3], v[162:165], v[108:111], v[0:3]\n"
      "v_mfma_f32_16x16x32_bf16 v[4:7], v[160:163], v[108:111], v[4:7]\n"
      "v_mfma_f32_16x16x32_bf16 v[0:3], v[162:165], v[112:115], v[0:3]\n"
      "v_mfma_f32_16x16x32_bf16 v[4:7], v[160:163], v[112:115], v[4:7]\n"
      "v_mfma_f32_16x16x32_bf16 v[0:3], v[162:165], v[116:119], v[0:3]\n"
      "v_mfma_f32_16x16x32_bf16 v[4:7], v[160:163], v[116:119], v[4:7]\n"
      "v_mfma_f32_16x16x32_bf16 v[24:27], v[162:165], v[124:127], v[24:27]\n"
      "v_mfma_f32_16x16x32_bf16 v[28:31], v[160:163], v[124:127], v[28:31]\n"
      "v_mfma_f32_16x16x32_bf16 v[24:27], v[162:165], v[128:131], v[24:27]\n"
      "v_mfma_f32_16x16x32_bf16 v[28:31], v[160:163], v[128:131], v[28:31]\n"
      "s_nop 2\n"
      "v_mul_f32 v0, v168, v0\n"
      "v_mul_f32 v1, v168, v1\n"
      "v_mfma_f32_16x16x32_bf16 v[24:27], v[162:165], v[132:135], v[24:27]\n"
      "v_mul_f32 v2, v168, v2\n"
      "v_mul_f32 v3, v168, v3\n"
      "v_mfma_f32_16x16x32_bf16 v[28:31], v[160:163], v[132:135], v[28:31]\n"
      "v_mul_f32 v4, v168, v4\n"
      "v_mul_f32 v5, v168, v5\n"
      "v_mfma_f32_16x16x32_bf16 v[24:27], v[162:165], v[136:139], v[24:27]\n"
      "v_mul_f32 v6, v168, v6\n"
      "v_mul_f32 v7, v168, v7\n"
      "v_mfma_f32_16x16x32_bf16 v[28:31], v[160:163], v[136:139], v[28:31]\n"
      "v_cvt_pk_bf16_f32 v144, v0, v1\n"
      "v_cvt_pk_bf16_f32 v145, v2, v3\n"
      "v_cvt_pk_bf16_f32 v146, v4, v5\n"
      "v_cvt_pk_bf16_f32 v147, v6, v7\n"
      "s_nop 1\n"
      "v_mfma_f32_16x16x32_bf16 v[32:35], a[0:3], v[144:147], v[32:35]\n"
      "v_mfma_f32_16x16x32_bf16 v[36:39], a[16:19], v[144:147], v[36:39]\n"
      "v_mfma_f32_16x16x32_bf16 v[40:43], a[32:35], v[144:147], v[40:43]\n"
      "v_mfma_f32_16x16x32_bf16 v[44:47], a[48:51], v[144:147], v[44:47]\n"
      "v_mul_f32 v24, v168, v24\n"
      "v_mul_f32 v25, v168, v25\n"
      "v_mul_f32 v26, v168, v26\n"
      "v_mfma_f32_16x16x32_bf16 v[48:51], a[64:67], v[144:147], v[48:51]\n"
      "v_mul_f32 v27, v168, v27\n"
      "v_mul_f32 v28, v168, v28\n"
      "v_mul_f32 v29, v168, v29\n"
      "v_mfma_f32_16x16x32_bf16 v[52:55], a[80:83], v[144:147], v[52:55]\n"
      "v_mul_f32 v30, v168, v30\n"
      "v_mul_f32 v31, v168, v31\n"
      "v_cvt_pk_bf16_f32 v156, v24, v25\n"
      "v_mfma_f32_16x16x32_bf16 v[56:59], a[96:99], v[144:147], v[56:59]\n"
      "v_cvt_pk_bf16_f32 v157, v26, v27\n"
      "v_cvt_pk_bf16_f32 v158, v28, v29\n"
      "v_cvt_pk_bf16_f32 v159, v30, v31\n"
      "v_mfma_f32_16x16x32_bf16 v[60:63], a[112:115], v[144:147], v[60:63]\n"
      "s_waitcnt lgkmcnt(0)\n"
      "v_mfma_f32_16x16x32_bf16 v[32:35], a[12:15], v[156:159], v[32:35]\n"
      "v_mfma_f32_16x16x32_bf16 v[36:39], a[28:31], v[156:159], v[36:39]\n"
      "v_mfma_f32_16x16x32_bf16 v[40:43], a[44:47], v[156:159], v[40:43]\n"
      "v_mfma_f32_16x16x32_bf16 v[44:47], a[60:63], v[156:159], v[44:47]\n"
      "v_mfma_f32_16x16x32_bf16 v[48:51], a[76:79], v[156:159], v[48:51]\n"
      "v_mfma_f32_16x16x32_bf16 v[52:55], a[92:95], v[156:159], v[52:55]\n"
      "v_mfma_f32_16x16x32_bf16 v[56:59], a[108:111], v[156:159], v[56:59]\n"
      "v_mfma_f32_16x16x32_bf16 v[60:63], a[124:127], v[156:159], v[60:63]\n"
      "s_waitcnt vmcnt(7)\n"
      "v_mfma_f32_16x16x32_bf16 v[8:11], v[162:165], v[64:67], v[8:11]\n"
      "v_mfma_f32_16x16x32_bf16 v[12:15], v[160:163], v[64:67], v[12:15]\n"
      "s_waitcnt vmcnt(6)\n"
      "v_mfma_f32_16x16x32_bf16 v[8:11], v[162:165], v[68:71], v[8:11]\n"
      "v_mfma_f32_16x16x32_bf16 v[12:15], v[160:163], v[68:71], v[12:15]\n"
      "s_waitcnt vmcnt(5)\n"
      "v_mfma_f32_16x16x32_bf16 v[8:11], v[162:165], v[72:75], v[8:11]\n"
      "v_mfma_f32_16x16x32_bf16 v[12:15], v[160:163], v[72:75], v[12:15]\n"
      "s_waitcnt vmcnt(4)\n"
      "v_mfma_f32_16x16x32_bf16 v[8:11], v[162:165], v[76:79], v[8:11]\n"
      "v_mfma_f32_16x16x32_bf16 v[12:15], v[160:163], v[76:79], v[12:15]\n"
      "s_waitcnt vmcnt(3)\n"
      "v_mfma_f32_16x16x32_bf16 v[16:19], v[162:165], v[84:87], v[16:19]\n"
      "v_mfma_f32_16x16x32_bf16 v[20:23], v[160:163], v[84:87], v[20:23]\n"
      "s_waitcnt vmcnt(2)\n"
      "v_mfma_f32_16x16x32_bf16 v[16:19], v[162:165], v[88:91], v[16:19]\n"
      "v_mfma_f32_16x16x32_bf16 v[20:23], v[160:163], v[88:91], v[20:23]\n"
      "s_nop 0\n"
      "v_mul_f32 v8, v168, v8\n"
      "v_mul_f32 v9, v168, v9\n"
      "s_waitcnt vmcnt(1)\n"
      "v_mfma_f32_16x16x32_bf16 v[16:19], v[162:165], v[92:95], v[16:19]\n"
      "v_mul_f32 v10, v168, v10\n"
      "v_mul_f32 v11, v168, v11\n"
      "v_mfma_f32_16x16x32_bf16 v[20:23], v[160:163], v[92:95], v[20:23]\n"
      "v_mul_f32 v12, v168, v12\n"
      "v_mul_f32 v13, v168, v13\n"
      "s_waitcnt vmcnt(0)\n"
      "v_mfma_f32_16x16x32_bf16 v[16:19], v[162:165], v[96:99], v[16:19]\n"
      "v_mul_f32 v14, v168, v14\n"
      "v_mul_f32 v15, v168, v15\n"
      "v_mfma_f32_16x16x32_bf16 v[20:23], v[160:163], v[96:99], v[20:23]\n"
      "v_cvt_pk_bf16_f32 v148, v8, v9\n"
      "v_cvt_pk_bf16_f32 v149, v10, v11\n"
      "v_cvt_pk_bf16_f32 v150, v12, v13\n"
      "v_cvt_pk_bf16_f32 v151, v14, v15\n"
      "s_nop 1\n"
      "v_mfma_f32_16x16x32_bf16 v[32:35], a[4:7], v[148:151], v[32:35]\n"
      "v_mfma_f32_16x16x32_bf16 v[36:39], a[20:23], v[148:151], v[36:39]\n"
      "v_mfma_f32_16x16x32_bf16 v[40:43], a[36:39], v[148:151], v[40:43]\n"
      "v_mfma_f32_16x16x32_bf16 v[44:47], a[52:55], v[148:151], v[44:47]\n"
      "v_mul_f32 v16, v168, v16\n"
      "v_mul_f32 v17, v168, v17\n"
      "v_mul_f32 v18, v168, v18\n"
      "v_mfma_f32_16x16x32_bf16 v[48:51], a[68:71], v[148:151], v[48:51]\n"
      "v_mul_f32 v19, v168, v19\n"
      "v_mul_f32 v20, v168, v20\n"
      "v_mul_f32 v21, v168, v21\n"
      "v_mfma_f32_16x16x32_bf16 v[52:55], a[84:87], v[148:151], v[52:55]\n"
      "v_mul_f32 v22, v168, v22\n"
      "v_mul_f32 v23, v168, v23\n"
      "v_cvt_pk_bf16_f32 v152, v16, v17\n"
      "v_mfma_f32_16x16x32_bf16 v[56:59], a[100:103], v[148:151], v[56:59]\n"
      "v_cvt_pk_bf16_f32 v153, v18, v19\n"
      "v_cvt_pk_bf16_f32 v154, v20, v21\n"
      "v_cvt_pk_bf16_f32 v155, v22, v23\n"
      "v_mfma_f32_16x16x32_bf16 v[60:63], a[116:119], v[148:151], v[60:63]\n"
      "s_nop 0\n"
      "v_mfma_f32_16x16x32_bf16 v[32:35], a[8:11], v[152:155], v[32:35]\n"
      "v_mfma_f32_16x16x32_bf16 v[36:39], a[24:27], v[152:155], v[36:39]\n"
      "v_mfma_f32_16x16x32_bf16 v[40:43], a[40:43], v[152:155], v[40:43]\n"
      "v_mfma_f32_16x16x32_bf16 v[44:47], a[56:59], v[152:155], v[44:47]\n"
      "v_mfma_f32_16x16x32_bf16 v[48:51], a[72:75], v[152:155], v[48:51]\n"
      "v_mfma_f32_16x16x32_bf16 v[52:55], a[88:91], v[152:155], v[52:55]\n"
      "v_mfma_f32_16x16x32_bf16 v[56:59], a[104:107], v[152:155], v[56:59]\n"
      "v_mfma_f32_16x16x32_bf16 v[60:63], a[120:123], v[152:155], v[60:63]\n"
      "s_cmp_lg_u32 %[stat], 0\n"
      "s_cbranch_scc1 8f\n"
      "v_cvt_pk_bf16_f32 v64, v32, v33\n"
      "v_cvt_pk_bf16_f32 v65, v34, v35\n"
      "v_cvt_pk_bf16_f32 v66, v36, v37\n"
      "v_cvt_pk_bf16_f32 v67, v38, v39\n"
      "buffer_store_dwordx4 v[64:67], v246, s[52:55], 0 offen offset:0 sc1\n"
      "v_cvt_pk_bf16_f32 v68, v40, v41\n"
      "v_cvt_pk_bf16_f32 v69, v42, v43\n"
      "v_cvt_pk_bf16_f32 v70, v44, v45\n"
      "v_cvt_pk_bf16_f32 v71, v46, v47\n"
      "buffer_store_dwordx4 v[68:71], v246, s[52:55], 0 offen offset:64 sc1\n"
      "v_cvt_pk_bf16_f32 v72, v48, v49\n"
      "v_cvt_pk_bf16_f32 v73, v50, v51\n"
      "v_cvt_pk_bf16_f32 v74, v52, v53\n"
      "v_cvt_pk_bf16_f32 v75, v54, v55\n"
      "buffer_store_dwordx4 v[72:75], v246, s[52:55], 0 offen offset:128 sc1\n"
      "v_cvt_pk_bf16_f32 v76, v56, v57\n"
      "v_cvt_pk_bf16_f32 v77, v58, v59\n"
      "v_cvt_pk_bf16_f32 v78, v60, v61\n"
      "v_cvt_pk_bf16_f32 v79, v62, v63\n"
      "buffer_store_dwordx4 v[76:79], v246, s[52:55], 0 offen offset:192 sc1\n"
      "s_endpgm\n"
      "12:\n"
      "s_cmp_eq_u32 s84, 0\n"
      "s_cbranch_scc1 13f\n"
      "ds_write_b32 v224, a187 offset:108\n"
      "ds_write_b16_d16_hi v225, a187 offset:54\n"
      "ds_write_b32 v224, a186 offset:104\n"
      "ds_write_b16_d16_hi v225, a186 offset:52\n"
      "ds_write_b32 v224, a185 offset:100\n"
      "ds_write_b16_d16_hi v225, a185 offset:50\n"
      "ds_write_b32 v224, a184 offset:96\n"
      "ds_write_b16_d16_hi v225, a184 offset:48\n"
      "ds_write_b32 v224, a183 offset:92\n"
      "ds_write_b16_d16_hi v225, a183 offset:46\n"
      "ds_write_b32 v224, a182 offset:88\n"
      "ds_write_b16_d16_hi v225, a182 offset:44\n"
      "ds_write_b32 v224, a181 offset:84\n"
      "ds_write_b16_d16_hi v225, a181 offset:42\n"
      "ds_write_b32 v224, a180 offset:80\n"
      "ds_write_b16_d16_hi v225, a180 offset:40\n"
      "13:\n"
      "ds_write_b32 v224, a179 offset:76\n"
      "ds_write_b16_d16_hi v225, a179 offset:38\n"
      "ds_write_b32 v224, a178 offset:72\n"
      "ds_write_b16_d16_hi v225, a178 offset:36\n"
      "ds_write_b32 v224, a177 offset:68\n"
      "ds_write_b16_d16_hi v225, a177 offset:34\n"
      "ds_write_b32 v224, a176 offset:64\n"
      "ds_write_b16_d16_hi v225, a176 offset:32\n"
      "s_branch 11b\n"
      "7:\n"
      "s_or_b32 %[stat], %[stat], 2\n"
      "s_waitcnt vmcnt(0) lgkmcnt(0)\n"
      "8:\n"
      "s_nop 7\n"
      ".Lk2_asm_end:\n"
      : [stat] "=&s"(stat), [t] "+{v247}"(t), "={v[32:35]}"(acc[0]), "={v[36:39]}"(acc[1]), "={v[40:43]}"(acc[2]), "={v[44:47]}"(acc[3]),
        "={v[48:51]}"(acc[4]), "={v[52:55]}"(acc[5]), "={v[56:59]}"(acc[6]), "={v[60:63]}"(acc[7])
      : [ka] "s"(__builtin_amdgcn_kernarg_segment_ptr()), [b] "s"(blockIdx.x)
      : "v0", "v1", "v2", "v3", "v4", "v5", "v6", "v7", "v8", "v9", "v10", "v11", "v12", "v13", "v14", "v15", "v16", "v17", "v18", "v19", "v20", "v21", "v22", "v23", "v24", "v25", "v26", "v27", "v28", "v29", "v30", "v31", "v64", "v65", "v66", "v67", "v68", "v69", "v70", "v71", "v72", "v73", "v74", "v75", "v76", "v77", "v78", "v79", "v80", "v81", "v82", "v83", "v84", "v85", "v86", "v87", "v88", "v89", "v90", "v91", "v92", "v93", "v94", "v95", "v96", "v97", "v98", "v99", "v100", "v101", "v102", "v103", "v104", "v105", "v106", "v107", "v108", "v109", "v110", "v111", "v112", "v113", "v114", "v115", "v116", "v117", "v118", "v119", "v120", "v121", "v122", "v123", "v124", "v125", "v126", "v127", "v128", "v129", "v130", "v131", "v132", "v133", "v134", "v135", "v136", "v137", "v138", "v139", "v140", "v141", "v142", "v143", "v144", "v145", "v146", "v147", "v148", "v149", "v150", "v151", "v152", "v153", "v154", "v155", "v156", "v157", "v158", "v159", "v160", "v161", "v162", "v163", "v164", "v165", "v166", "v167", "v168", "v169", "v170", "v171", "v172", "v173", "v174", "v175", "v176", "v177", "v178", "v179", "v180", "v181", "v182", "v183", "v184", "v185", "v186", "v187", "v188", "v189", "v190", "v191", "v192", "v193", "v194", "v195", "v196", "v197", "v198", "v199", "v200", "v201", "v202", "v203", "v204", "v205", "v206", "v207", "v208", "v209", "v210", "v211", "v212", "v213", "v214", "v215", "v216", "v217", "v218", "v219", "v220", "v221", "v222", "v223", "v224", "v225", "v226", "v227", "v228", "v229", "v230", "v231", "v232", "v233", "v234", "v235", "v236", "v237", "v238", "v239", "v240", "v241", "v242", "v243", "v244", "v245", "v246", "v248", "v249", "v250", "v251", "v252", "v253", "v254", "v255", "a0", "a1", "a2", "a3", "a4", "a5", "a6", "a7", "a8", "a9", "a10", "a11", "a12", "a13", "a14", "a15", "a16", "a17", "a18", "a19", "a20", "a21", "a22", "a23", "a24", "a25", "a26", "a27", "a28", "a29", "a30", "a31", "a32", "a33", "a34", "a35", "a36", "a37", "a38", "a39", "a40", "a41", "a42", "a43", "a44", "a45", "a46", "a47", "a48", "a49", "a50", "a51", "a52", "a53", "a54", "a55", "a56", "a57", "a58", "a59", "a60", "a61", "a62", "a63", "a64", "a65", "a66", "a67", "a68", "a69", "a70", "a71", "a72", "a73", "a74", "a75", "a76", "a77", "a78", "a79", "a80", "a81", "a82", "a83", "a84", "a85", "a86", "a87", "a88", "a89", "a90", "a91", "a92", "a93", "a94", "a95", "a96", "a97", "a98", "a99", "a100", "a101", "a102", "a103", "a104", "a105", "a106", "a107", "a108", "a109", "a110", "a111", "a112", "a113", "a114", "a115", "a116", "a117", "a118", "a119", "a120", "a121", "a122", "a123", "a124", "a125", "a126", "a127", "a128", "a129", "a130", "a131", "a132", "a133", "a134", "a135", "a136", "a137", "a138", "a139", "a140", "a141", "a142", "a143", "a144", "a145", "a146", "a147", "a148", "a149", "a150", "a151", "a152", "a153", "a154", "a155", "a156", "a157", "a158", "a159", "a160", "a161", "a162", "a163", "a164", "a165", "a166", "a167", "a168", "a169", "a170", "a171", "a172", "a173", "a174", "a175", "a176", "a177", "a178", "a179", "a180", "a181", "a182", "a183", "a184", "a185", "a186", "a187", "a188", "a189", "a190", "a191", "a192", "a193", "a194", "a195", "a196", "a197", "a198", "a199", "a200", "a201", "a202", "a203", "a204", "a205", "a206", "a207", "a208", "a209", "a210", "a211", "a212", "a213", "a214", "a215", "a216", "a217", "a218", "a219", "a220", "a221", "a222", "a223", "a224", "s40", "s41", "s42", "s43", "s44", "s45", "s46", "s47", "s48", "s49", "s50", "s51", "s52", "s53", "s54", "s55", "s56", "s57", "s58", "s59", "s60", "s61", "s62", "s63", "s64", "s65", "s66", "s67", "s68", "s69", "s70", "s71", "s72", "s73", "s74", "s75", "s76", "s77", "s78", "s79", "s80", "s81", "s82", "s83", "s84", "s85", "s86", "s87", "s88", "s89", "s90", "s91", "s92", "s93", "vcc", "scc", "memory");
  if (stat == 0) return;
  const unsigned l = t & 63, w = __builtin_amdgcn_readfirstlane(t >> 6), b = blockIdx.x;
  unsigned qb, split;
  if (b < 240) { const unsigned i = b >> 3; qb = (b & 7) * 6 + i / 5; split = i % 5; } else { qb = 48; split = b - 240; }
  const unsigned own = split * 4 + w, i = l & 15, g = l >> 4, a4 = i >> 2, b4 = i & 3;
  const unsigned lr = own * 13 + i, row = qb * 256 + lr;
  const bool rok = i < 13 && lr < 256 && row < (unsigned)NN;
  const unsigned oo = rok ? (row >> 4) * 4096 + ((row & 15) + 16 * g) * 16 : OOB, xo = rok ? row * 256 + g * 16 : OOB;
  const unsigned to = (l & 7) < 7 ? (unsigned)OFF_T + (qb * 5 + split) * 1024 + l * 16 + w * 4 : OOB;
  const rsrc_t rh = mkrs(ws + OFF_H, NN * 256u), rw = mkrs(ws, 32u << 20);
  const unsigned tb = L_TILE + w * 8192;
  const unsigned lp = 16 * (i ^ ((g & 1) << 1));
  const unsigned ta = tb + 256 * (4 * a4 + g) + 16 * (b4 >> 1) + 8 * (b4 & 1), tx = (a4 << 1) | (g & 1);
  f32x4 og[8];
  auto consume = [&](int buf, u32x4 s) {
#pragma unroll
    for (int c = 0; c < 8; ++c) {
      const unsigned o = ta + 32 * (c ^ tx) + buf * 32768;
      const u32x2 v0 = trd(sm, o), v1 = trd(sm, o + 4096);
      og[c] = mfma((u32x4){v0[0], v0[1], v1[0], v1[1]}, s, og[c]);
    }
  };
  auto project = [&](unsigned wbase, float scale) {
    u32x4 bv[4];
#pragma unroll
    for (int s = 0; s < 4; ++s) bv[s] = pk8(og[2 * s] * scale, og[2 * s + 1] * scale);
#pragma unroll
    for (int et = 0; et < 8; ++et)
#pragma unroll
      for (int s = 0; s < 4; ++s) acc[et] = mfma(*(const u32x4*)(sm + wbase + (wbase && et >= 4 ? L_W3 - 49152 : 0u) + (et * 4 + s) * 1024 + l * 16), bv[s], acc[et]);
  };
  if (stat & 1) {
    unsigned* list = (unsigned*)(sm + L_LIST + w * LIST_B);
    const unsigned tv = __builtin_amdgcn_raw_buffer_load_b32(rw, (int)to, 0, 0), len = tv >> 18, e0 = (unsigned)OFF_E + (tv & 0xffffu) + ((l & 7) * 8 + (l >> 3)) * (CH * 4);
#pragma unroll
    for (int c = 0; c < 8; ++c) og[c] = (f32x4){0.f, 0.f, 0.f, 0.f};
    auto issue = [&](unsigned k, int buf) -> u32x4 {
      const u32x4 ea = *(const u32x4*)(list + 32 * k + 8 * g), eb = *(const u32x4*)(list + 32 * k + 8 * g + 4);
      asm volatile("s_waitcnt lgkmcnt(0)" ::: "memory");
#pragma unroll
      for (int p = 0; p < 8; ++p) dma16(rh, (((p < 4 ? ea[p & 3] : eb[p & 3]) & 0xffffu) << 8) | (lp ^ (64 * (p & 3))), tb + buf * 32768 + p * 1024);
      u32x4 s;
#pragma unroll
      for (int j = 0; j < 2; ++j) {
        s[j] = (((ea[2 * j] >> (16 + i)) & 1u) | (((ea[2 * j + 1] >> (16 + i)) & 1u) << 16)) * 0x3f80u;
        s[2 + j] = (((eb[2 * j] >> (16 + i)) & 1u) | (((eb[2 * j + 1] >> (16 + i)) & 1u) << 16)) * 0x3f80u;
      }
      return s;
    };
    for (unsigned base = 0;; base += 16) {
      u32x4 ev[4];
#pragma unroll
      for (int k = 0; k < 4; ++k) ev[k] = ld16(rw, e0 + base * 4 + 16 * k);
      const unsigned n = len > base ? min(len - base, 16u) : 0u;
      const unsigned inc = scan64(n), pos = inc - n, T = __builtin_amdgcn_readlane(inc, 63), nt = (T + 31) >> 5;
#pragma unroll
      for (int k = 0; k < 16; ++k)
        if ((unsigned)k < n) list[pos + k] = ev[k >> 2][k & 3];
      list[T + l] = INVE;
      u32x4 s0 = {0u, 0u, 0u, 0u}, s1 = s0;
      if (nt > 0) s0 = issue(0, 0);
      if (nt > 1) s1 = issue(1, 1);
      for (unsigned k = 0; k < nt; k += 2) {
        if (k + 1 < nt) asm volatile("s_waitcnt vmcnt(8)" ::: "memory"); else asm volatile("s_waitcnt vmcnt(0)" ::: "memory");
        consume(0, s0);
        if (k + 2 < nt) s0 = issue(k + 2, 0);
        if (k + 1 >= nt) break;
        if (k + 2 < nt) asm volatile("s_waitcnt vmcnt(8)" ::: "memory"); else asm volatile("s_waitcnt vmcnt(0)" ::: "memory");
        consume(1, s1);
        if (k + 3 < nt) s1 = issue(k + 3, 1);
      }
      if (!__any(len > base + 16)) break;
    }
    project(32768, rok ? bflo(deg_inv[row]) : 0.f);
  }
  if (stat & 2) {
    u32x4 qf[4];
#pragma unroll
    for (int s = 0; s < 4; ++s) qf[s] = ld16(rw, (unsigned)OFF_Q + oo + s * 1024);
    if (g == 3) qf[3][3] &= 0xffffu;
    float m = -3.0e38f;
#pragma unroll
    for (int c = 0; c < 8; ++c) og[c] = (f32x4){0.f, 0.f, 0.f, 0.f};
#pragma unroll 1
    for (unsigned k0 = 0; k0 < (unsigned)NN; k0 += 32) {
      asm volatile("s_waitcnt lgkmcnt(0)" ::: "memory");
#pragma unroll
      for (int p = 0; p < 8; ++p) dma16(rh, (k0 + 4 * p + g) * 256 + (lp ^ (64 * (p & 3))), tb + p * 1024);
      asm volatile("s_waitcnt vmcnt(0)" ::: "memory");
      f32x4 sc[2];
      float mx = m;
#pragma unroll
      for (int tt = 0; tt < 2; ++tt) {
        sc[tt] = (f32x4){0.f, 0.f, 0.f, 0.f};
#pragma unroll
        for (int s = 0; s < 4; ++s)
          sc[tt] = mfma(*(const u32x4*)(sm + tb + 256 * (16 * tt + 4 * b4 + a4) + 16 * ((4 * s + g) ^ ((b4 << 2) | ((a4 & 1) << 1)))), qf[s], sc[tt]);
#pragma unroll
        for (int r = 0; r < 4; ++r) {
          if (k0 + 16 * tt + 4 * r + g >= (unsigned)NN) sc[tt][r] = -3.0e38f;
          mx = fmaxf(mx, sc[tt][r]);
        }
      }
      mx = max_g(mx);
      const float al = __builtin_amdgcn_exp2f(m - mx);
      m = mx;
#pragma unroll
      for (int tt = 0; tt < 2; ++tt)
#pragma unroll
        for (int r = 0; r < 4; ++r) sc[tt][r] = __builtin_amdgcn_exp2f(sc[tt][r] - mx);
#pragma unroll
      for (int c = 0; c < 8; ++c) og[c] *= al;
      consume(0, pk8(sc[0], sc[1]));
    }
    project(0, 1.0f / sum_g(g == 3 ? og[7][3] : 0.f));
  }
#pragma unroll
  for (int u = 0; u < 4; ++u) __builtin_amdgcn_raw_buffer_store_b128(pk8(acc[2 * u], acc[2 * u + 1]), mkrs(out, NN * 256u), (int)(xo + u * 64), 0, 0);
}

extern "C" void kernel_launch(void* const* d_in, const int* in_sizes, int n_in, void* d_out, int out_size, void* d_ws, size_t ws_size,
                              hipStream_t stream) {
  const unsigned short* x = (const unsigned short*)d_in[0];
  const int* src = (const int*)d_in[1];
  const int* dst = (const int*)d_in[2];
  const unsigned short* dinv = (const unsigned short*)d_in[3];
  const unsigned short* wm = (const unsigned short*)d_in[4];
  const unsigned short* wq = (const unsigned short*)d_in[5];
  const unsigned short* wk = (const unsigned short*)d_in[6];
  const unsigned short* wv = (const unsigned short*)d_in[7];
  const unsigned short* wo = (const unsigned short*)d_in[8];
  char* ws = (char*)d_ws;
  hipLaunchKernelGGL(k1_prep, dim3(NCH + 4 + NQT / 4), dim3(256), 0, stream, x, src, dst, wm, wq, wk, wv, wo, ws);
  hipLaunchKernelGGL(k2_attn, dim3(245), dim3(256), 0, stream, x, dinv, ws, (unsigned short*)d_out);
}
```
